```python
import torch

def _softmax(x, axis=-1):
    return torch.softmax(x, dim=axis)

def _gelu(x):
    return torch.nn.functional.gelu(x, approximate='tanh')

def _silu(x):
    return torch.nn.functional.silu(x)

def _sigmoid(x):
    return torch.sigmoid(x)

def _layernorm(x, axis=-1, eps=1e-05):
    m = x.mean(dim=axis, keepdim=True)
    v = x.var(dim=axis, keepdim=True, unbiased=False)
    return (x - m) / torch.sqrt(v + eps)

def _scatter_add(tgt, idx, src, axis=0):
    return tgt.clone().index_add_(axis, idx, src)

def _segment_sum(src, seg, n):
    out = torch.zeros((n,) + src.shape[1:], dtype=src.dtype)
    return out.index_add_(0, seg, src)

def _pad(x, pw):
    flat = tuple((v for pair in reversed(pw) for v in pair))
    return torch.nn.functional.pad(x, flat)
kernel_name = 'standalone__factorization_afm__recsys'

def setup_inputs():

    _w = lambda a: (lambda t: t.to(torch.bfloat16) if t.is_floating_point() else t)(torch.as_tensor(a))
    B, F, D, V = (4096, 26, 128, min(1000000, 100000))
    A = D
    ii, jj = tuple(torch.triu_indices(F, F, 1).unbind(0))
    return {'idx': _w((lambda lo, hi, *s: torch.randint(lo, hi, s))(0, V, B, F).to(torch.int64)), 'table': _w((lambda *s: torch.randn(*s, dtype=torch.bfloat16) * 0.2)(V, D)), 'ii': _w(ii.to(torch.int64)), 'jj': _w(jj.to(torch.int64)), 'W': _w((lambda *s: torch.randn(*s, dtype=torch.bfloat16) * 1.6)(D, A) * (1.0 / D ** 0.5)), 'b': _w((lambda *s: torch.randn(*s, dtype=torch.bfloat16) * 1.6)(A)), 'h': _w((lambda *s: torch.randn(*s, dtype=torch.bfloat16) * 1.6)(A)), 'p': _w((lambda *s: torch.randn(*s, dtype=torch.bfloat16) * 0.2)(D))}

def reference(idx, table, ii, jj, W, b, h, p):
    emb = table[idx]
    ei = emb[:, ii, :].clone()
    ej = emb[:, jj, :].clone()
    pair = ei * ej
    hid = pair @ W + b
    hid = 0.5 * (hid + torch.abs(hid))
    e = hid @ h
    alpha = _softmax(e, axis=-1)
    pooled = torch.einsum('bp,bpd->bd', alpha, pair)
    return pooled @ p
```

```cpp
#include <hip/hip_runtime.h>
#include <hip/hip_bf16.h>
#include <stdint.h>

static __device__ __forceinline__ float bf2f(unsigned short x) { return __uint_as_float(((unsigned)x) << 16); }
static __device__ __forceinline__ unsigned short f2bf(float f) {
  unsigned u = __float_as_uint(f);
  return (unsigned short)((u + 0x7FFF + ((u >> 16) & 1)) >> 16);
}

__global__ __launch_bounds__(256) void afm_naive(const int* __restrict__ idx, const unsigned short* __restrict__ table,
    const int* __restrict__ ii, const int* __restrict__ jj, const unsigned short* __restrict__ W,
    const unsigned short* __restrict__ bb, const unsigned short* __restrict__ hh, const unsigned short* __restrict__ pp,
    unsigned short* __restrict__ out) {
  __shared__ float E[26][128];
  __shared__ float e[325];
  __shared__ float s[325];
  __shared__ float red[256];
  int b = blockIdx.x, t = threadIdx.x;
  for (int n = t; n < 26 * 128; n += 256) {
    int f = n >> 7, d = n & 127;
    E[f][d] = bf2f(table[(size_t)idx[b * 26 + f] * 128 + d]);
  }
  __syncthreads();
  for (int p = t; p < 325; p += 256) {
    int i = ii[p], j = jj[p];
    float acc_e = 0.f, sp = 0.f;
    for (int a = 0; a < 128; ++a) {
      float x = 0.f;
      for (int d = 0; d < 128; ++d) x += bf2f(f2bf(E[i][d] * E[j][d])) * bf2f(W[d * 128 + a]);
      x += bf2f(bb[a]);
      x = x > 0.f ? x : 0.f;
      acc_e += x * bf2f(hh[a]);
    }
    for (int d = 0; d < 128; ++d) sp += bf2f(f2bf(E[i][d] * E[j][d])) * bf2f(pp[d]);
    e[p] = acc_e; s[p] = sp;
  }
  __syncthreads();
  if (t == 0) {
    float m = -INFINITY;
    for (int p = 0; p < 325; ++p) m = fmaxf(m, e[p]);
    float l = 0.f, o = 0.f;
    for (int p = 0; p < 325; ++p) { float w = __expf(e[p] - m); l += w; o += w * s[p]; }
    out[b] = f2bf(o / l);
  }
}

extern "C" void kernel_launch(void* const* d_in, const int* in_sizes, int n_in,
                              void* d_out, int out_size, void* d_ws, size_t ws_size,
                              hipStream_t stream) {
  afm_naive<<<4096, 256, 0, stream>>>((const int*)d_in[0], (const unsigned short*)d_in[1], (const int*)d_in[2], (const int*)d_in[3],
    (const unsigned short*)d_in[4], (const unsigned short*)d_in[5], (const unsigned short*)d_in[6], (const unsigned short*)d_in[7],
    (unsigned short*)d_out);
}
```

```cpp
#include <hip/hip_runtime.h>
#include <hip/hip_bf16.h>
#include <stdint.h>

static __device__ __forceinline__ float bf2f(unsigned short x) { return __uint_as_float(((unsigned)x) << 16); }
static __device__ __forceinline__ float rbf(float f) {
  unsigned u = __float_as_uint(f);
  u = (u + 0x7FFFu + ((u >> 16) & 1u)) & 0xFFFF0000u;
  return __uint_as_float(u);
}

static __device__ __forceinline__ float wave_sum(float v) {
  for (int k = 32; k >= 1; k >>= 1) v += __shfl_xor(v, k, 64);
  return v;
}
static __device__ __forceinline__ void wave_lds_sync() {
  asm volatile("s_waitcnt lgkmcnt(0)" ::: "memory");
  __builtin_amdgcn_wave_barrier();
}
static __device__ __forceinline__ void afm_general(const int* __restrict__ idx, const unsigned short* __restrict__ table,
                                                const unsigned short* __restrict__ W, const unsigned short* __restrict__ bv,
                                                const unsigned short* __restrict__ hv, const unsigned short* __restrict__ pv,
                                                unsigned short* __restrict__ out, int lane, float* sm) {
  float* E = sm;
  float* P = sm + 26 * 128;
  const unsigned* W2 = (const unsigned*)W;
  const float b0 = bf2f(bv[2 * lane]), b1 = bf2f(bv[2 * lane + 1]);
  const float h0 = bf2f(hv[2 * lane]), h1 = bf2f(hv[2 * lane + 1]);
  const float p0 = bf2f(pv[2 * lane]), p1 = bf2f(pv[2 * lane + 1]);
  for (int n = 0; n < 4; ++n) {
    wave_lds_sync();
    for (int f = 0; f < 26; ++f) {
      const unsigned v = ((const unsigned*)(table + (size_t)idx[n * 26 + f] * 128))[lane];
      E[f * 128 + 2 * lane] = __uint_as_float(v << 16);
      E[f * 128 + 2 * lane + 1] = __uint_as_float(v & 0xffff0000u);
    }
    float m = -INFINITY, l = 0.f, o = 0.f;
    for (int i = 0; i < 25; ++i) {
      for (int j = i + 1; j < 26; ++j) {
        wave_lds_sync();
        const float q0 = rbf(E[i * 128 + 2 * lane] * E[j * 128 + 2 * lane]);
        const float q1 = rbf(E[i * 128 + 2 * lane + 1] * E[j * 128 + 2 * lane + 1]);
        P[2 * lane] = q0;
        P[2 * lane + 1] = q1;
        wave_lds_sync();
        float x0 = 0.f, x1 = 0.f;
#pragma unroll 8
        for (int d = 0; d < 128; ++d) {
          const unsigned w2 = W2[d * 64 + lane];
          const float q = P[d];
          x0 = fmaf(q, __uint_as_float(w2 << 16), x0);
          x1 = fmaf(q, __uint_as_float(w2 & 0xffff0000u), x1);
        }
        x0 += b0; x1 += b1;
        const float e = wave_sum(fmaxf(x0, 0.f) * h0 + fmaxf(x1, 0.f) * h1);
        const float s = wave_sum(q0 * p0 + q1 * p1);
        const float mn = fmaxf(m, e);
        const float c = __expf(m - mn), w = __expf(e - mn);
        l = l * c + w; o = o * c + w * s; m = mn;
      }
    }
    if (lane == 0) {
      const float r = o / l;
      unsigned u = __float_as_uint(r);
      out[n] = (r != r) ? (unsigned short)0x7FC0 : (unsigned short)((u + 0x7FFFu + ((u >> 16) & 1u)) >> 16);
    }
  }
}

__global__ __launch_bounds__(256, 1) void afm_fused(const int* __restrict__ idx, const unsigned short* __restrict__ table,
                                                    const unsigned short* __restrict__ W, const unsigned short* __restrict__ bv,
                                                    const unsigned short* __restrict__ hv, const unsigned short* __restrict__ pv,
                                                    unsigned short* __restrict__ out, unsigned long long tstride) {
  __shared__ __attribute__((aligned(16))) unsigned char lds[163840];
  const int w = __builtin_amdgcn_readfirstlane(threadIdx.x >> 6);
  const unsigned blk = blockIdx.x;
  unsigned flag;
  const unsigned ldsbase = (unsigned)(size_t)(__attribute__((address_space(3))) unsigned char*)lds;
  asm volatile(
      ".Lstart_%=:\n"
      "s_load_dwordx8 s[36:43], %1, 0x0\n"
      "s_load_dwordx4 s[44:47], %1, 0x20\n"
      "s_load_dwordx2 s[48:49], %1, 0x30\n"
      "s_load_dword s93, %1, 0x38\n"
      "s_mov_b32 s50, %3\n"
      "v_mbcnt_lo_u32_b32 v207, -1, 0\n"
      "s_mul_i32 s52, %2, 1664\n"
      "v_mbcnt_hi_u32_b32 v207, -1, v207\n"
      "s_mul_i32 s53, s50, 416\n"
      "v_lshrrev_b32 v249, 4, v207\n"
      "s_add_u32 s52, s52, s53\n"
      "v_mul_u32_u24 v253, 104, v249\n"
      "s_lshl_b32 s54, %2, 5\n"
      "s_mov_b32 s51, 0\n"
      "s_mov_b32 s69, 0xffff0000\n"
      "v_and_b32 v248, 15, v207\n"
      "s_waitcnt lgkmcnt(0)\n"
      "s_add_u32 s36, s36, s52\n"
      "s_addc_u32 s37, s37, 0\n"
      "global_load_dwordx4 v[112:115], v253, s[36:37] offset:0\n"
      "global_load_dwordx4 v[116:119], v253, s[36:37] offset:16\n"
      "global_load_dwordx4 v[120:123], v253, s[36:37] offset:32\n"
      "global_load_dwordx4 v[124:127], v253, s[36:37] offset:48\n"
      "global_load_dwordx4 v[128:131], v253, s[36:37] offset:64\n"
      "global_load_dwordx4 v[132:135], v253, s[36:37] offset:80\n"
      "global_load_dwordx2 v[136:137], v253, s[36:37] offset:96\n"
      "v_lshlrev_b32 v250, 4, v248\n"
      "s_lshl_b32 s52, s50, 10\n"
      "global_load_dwordx4 v[56:59], v250, s[44:45]\n"
      "global_load_dwordx4 v[60:63], v250, s[42:43]\n"
      "global_load_dwordx4 v[64:67], v250, s[46:47]\n"
      "v_lshl_add_u32 v242, v249, 8, v250\n"
      "v_add_u32 v242, s52, v242\n"
      "global_load_dwordx4 v[0:3], v242, s[40:41]\n"
      "s_add_u32 s40, s40, 4096\n"
      "s_addc_u32 s41, s41, 0\n"
      "global_load_dwordx4 v[4:7], v242, s[40:41]\n"
      "s_add_u32 s40, s40, 4096\n"
      "s_addc_u32 s41, s41, 0\n"
      "global_load_dwordx4 v[8:11], v242, s[40:41]\n"
      "s_add_u32 s40, s40, 4096\n"
      "s_addc_u32 s41, s41, 0\n"
      "global_load_dwordx4 v[12:15], v242, s[40:41]\n"
      "s_add_u32 s40, s40, 4096\n"
      "s_addc_u32 s41, s41, 0\n"
      "global_load_dwordx4 v[16:19], v242, s[40:41]\n"
      "s_add_u32 s40, s40, 4096\n"
      "s_addc_u32 s41, s41, 0\n"
      "global_load_dwordx4 v[20:23], v242, s[40:41]\n"
      "s_add_u32 s40, s40, 4096\n"
      "s_addc_u32 s41, s41, 0\n"
      "global_load_dwordx4 v[24:27], v242, s[40:41]\n"
      "s_add_u32 s40, s40, 4096\n"
      "s_addc_u32 s41, s41, 0\n"
      "global_load_dwordx4 v[28:31], v242, s[40:41]\n"
      "s_getpc_b64 s[56:57]\n"
      ".Lpc_%=:\n"
      "s_sub_u32 s56, s56, .Lpc_%=-.Lstart_%=\n"
      "s_subb_u32 s57, s57, 0\n"
      "s_lshl_b32 s52, s50, 13\n"
      "v_lshlrev_b32 v243, 7, v207\n"
      "v_add_u32 v243, s52, v243\n"
      "s_mov_b32 s53, .Lend_%=-.Lstart_%=-4\n"
      "v_min_u32 v243, s53, v243\n"
      "global_load_dword v253, v243, s[56:57]\n"
      "s_add_u32 s48, s48, s54\n"
      "s_addc_u32 s49, s49, 0\n"
      "s_lshr_b32 s52, %2, 3\n"
      "s_and_b32 s52, s52, 31\n"
      "s_lshl_b32 s52, s52, 2\n"
      "s_add_u32 s52, s52, s50\n"
      "s_lshl_b32 s52, s52, 1\n"
      "v_lshrrev_b32 v244, 5, v207\n"
      "v_add_u32 v244, s52, v244\n"
      "v_mul_u32_u24 v244, s93, v244\n"
      "global_load_dword v253, v244, s[38:39]\n"
      "v_mul_u32_u24 v200, 7072, v248\n"
      "v_mul_u32_u24 v203, 2928, v248\n"
      "v_lshl_add_u32 v200, v249, 4, v200\n"
      "v_add_u32 v203, 113152, v203\n"
      "v_mov_b32 v245, 0x1540150\n"
      "v_mov_b32 v246, 0xec000c\n"
      "v_cmp_lt_u32 vcc, 1, v249\n"
      "v_and_b32 v247, 1, v249\n"
      "v_lshlrev_b32 v247, 4, v247\n"
      "v_cndmask_b32 v245, v245, v246, vcc\n"
      "v_lshrrev_b32 v245, v247, v245\n"
      "v_and_b32 v245, 0xffff, v245\n"
      "v_add_u32 v204, v245, v203\n"
      "v_mov_b32 v245, 0x80004\n"
      "v_mov_b32 v246, 0x7c0078\n"
      "v_cmp_lt_u32 vcc, 1, v249\n"
      "v_and_b32 v247, 1, v249\n"
      "v_lshlrev_b32 v247, 4, v247\n"
      "v_cndmask_b32 v245, v245, v246, vcc\n"
      "v_lshrrev_b32 v245, v247, v245\n"
      "v_and_b32 v245, 0xffff, v245\n"
      "v_add_u32 v205, v245, v203\n"
      "v_mul_u32_u24 v244, 112, v249\n"
      "s_mul_i32 s52, s50, 28288\n"
      "v_add_u32 v203, v244, v203\n"
      "v_mul_u32_u24 v206, 7072, v249\n"
      "v_add_u32 v206, v250, v206\n"
      "v_add_u32 v206, s52, v206\n"
      "v_mov_b32 v246, 0\n"
      "v_mov_b32 v247, 160704\n"
      "ds_write_b32 v247, v246\n"
      "ds_write_b32 v247, v246 offset:4\n"
      "v_lshrrev_b32 v243, 2, v248\n"
      "v_and_b32 v247, 3, v248\n"
      "v_lshl_add_u32 v243, v249, 3, v243\n"
      "v_and_b32 v244, 1, v249\n"
      "v_lshlrev_b32 v244, 2, v244\n"
      "v_lshlrev_b32 v247, 3, v247\n"
      "v_add_u32 v245, v243, v244\n"
      "v_xor_b32 v244, 4, v244\n"
      "v_add_u32 v246, v243, v244\n"
      "v_mul_u32_u24 v78, 288, v245\n"
      "v_mul_u32_u24 v79, 288, v246\n"
      "v_add_u32 v247, 113152, v247\n"
      "v_add_u32 v78, v247, v78\n"
      "v_add_u32 v79, v247, v79\n"
      "s_mul_i32 s52, s50, 28288\n"
      "v_mul_u32_u24 v160, 272, v248\n"
      "v_add_u32 v161, 16, v248\n"
      "v_lshl_add_u32 v160, v249, 4, v160\n"
      "v_min_u32 v161, 25, v161\n"
      "v_add_u32 v160, s52, v160\n"
      "v_mul_u32_u24 v161, 272, v161\n"
      "v_lshl_add_u32 v161, v249, 4, v161\n"
      "v_add_u32 v161, s52, v161\n"
      "v_accvgpr_write_b32 a168, v160\n"
      "v_accvgpr_write_b32 a169, v161\n"
      "s_mul_i32 s52, s50, 11712\n"
      "s_add_u32 s52, s52, 113152\n"
      "s_add_u32 s53, s52, 2912\n"
      "v_mul_u32_u24 v160, 112, v248\n"
      "v_cmp_eq_u32 vcc, 3, v249\n"
      "v_lshl_add_u32 v160, v249, 4, v160\n"
      "v_mov_b32 v162, s53\n"
      "v_add_u32 v160, s52, v160\n"
      "v_add_u32 v161, 64, v160\n"
      "v_cndmask_b32 v161, v161, v162, vcc\n"
      "v_accvgpr_write_b32 a170, v160\n"
      "v_accvgpr_write_b32 a171, v161\n"
      "v_cmp_ge_u32 s[54:55], 9, v248\n"
      "s_add_u32 s52, s52, 1856\n"
      "v_mul_u32_u24 v160, 448, v249\n"
      "v_lshl_add_u32 v160, v248, 2, v160\n"
      "v_add_u32 v160, s52, v160\n"
      "v_lshl_add_u32 v163, v249, 2, 0\n"
      "v_add_u32 v161, 0, v160\n"
      "v_cmp_lt_u32 vcc, v163, v248\n"
      "s_nop 0\n"
      "s_and_b64 vcc, vcc, s[54:55]\n"
      "s_nop 1\n"
      "v_cndmask_b32 v161, v162, v161, vcc\n"
      "v_accvgpr_write_b32 a172, v161\n"
      "v_lshl_add_u32 v163, v249, 2, 1\n"
      "v_add_u32 v161, 112, v160\n"
      "v_cmp_lt_u32 vcc, v163, v248\n"
      "s_nop 0\n"
      "s_and_b64 vcc, vcc, s[54:55]\n"
      "s_nop 1\n"
      "v_cndmask_b32 v161, v162, v161, vcc\n"
      "v_accvgpr_write_b32 a173, v161\n"
      "v_lshl_add_u32 v163, v249, 2, 2\n"
      "v_add_u32 v161, 224, v160\n"
      "v_cmp_lt_u32 vcc, v163, v248\n"
      "s_nop 0\n"
      "s_and_b64 vcc, vcc, s[54:55]\n"
      "s_nop 1\n"
      "v_cndmask_b32 v161, v162, v161, vcc\n"
      "v_accvgpr_write_b32 a174, v161\n"
      "v_lshl_add_u32 v163, v249, 2, 3\n"
      "v_add_u32 v161, 336, v160\n"
      "v_cmp_lt_u32 vcc, v163, v248\n"
      "s_nop 0\n"
      "s_and_b64 vcc, vcc, s[54:55]\n"
      "s_nop 1\n"
      "v_cndmask_b32 v161, v162, v161, vcc\n"
      "v_accvgpr_write_b32 a175, v161\n"
      "v_lshlrev_b32 v160, 4, v249\n"
      "s_lshl_b32 s53, s50, 4\n"
      "v_add_u32 v160, 160000, v160\n"
      "s_add_u32 s53, s53, 160512\n"
      "v_lshlrev_b32 v161, 2, v249\n"
      "v_add_u32 v161, s53, v161\n"
      "v_accvgpr_write_b32 a176, v160\n"
      "v_accvgpr_write_b32 a177, v161\n"
      "v_lshl_add_u32 v160, s50, 2, v249\n"
      "v_mul_u32_u24 v161, 112, v248\n"
      "v_mul_u32_u24 v160, 2928, v160\n"
      "s_lshl_b32 s53, s50, 3\n"
      "v_add_u32 v160, 113152, v160\n"
      "v_lshlrev_b32 v162, 1, v249\n"
      "v_add_u32 v161, v160, v161\n"
      "v_add_u32 v162, s53, v162\n"
      "v_accvgpr_write_b32 a178, v160\n"
      "v_accvgpr_write_b32 a179, v161\n"
      "v_accvgpr_write_b32 a180, v162\n"
      "s_cmp_eq_u32 s50, 1\n"
      "s_cbranch_scc1 .Ldesc1_%=\n"
      "s_cmp_eq_u32 s50, 2\n"
      "s_cbranch_scc1 .Ldesc2_%=\n"
      "s_cmp_eq_u32 s50, 3\n"
      "s_cbranch_scc1 .Ldesc3_%=\n"
      "s_mov_b32 s70, 0xa40008\n"
      "s_mov_b32 s71, 0x109\n"
      "s_mov_b32 s72, 0x40012a\n"
      "s_mov_b32 s73, 0x14b\n"
      "s_mov_b32 s74, 0x16c\n"
      "s_mov_b32 s75, 0x18d\n"
      "s_mov_b32 s76, 0x1ae\n"
      "s_mov_b32 s77, 0x1cf\n"
      "s_mov_b32 s78, 0x10001f0\n"
      "s_mov_b32 s79, 0x211\n"
      "s_mov_b32 s80, 0x232\n"
      "s_mov_b32 s81, 0x253\n"
      "s_mov_b32 s82, 0x274\n"
      "s_mov_b32 s83, 0x295\n"
      "s_mov_b32 s84, 0x2b6\n"
      "s_mov_b32 s85, 0x2d7\n"
      "s_mov_b32 s86, 0x2f8\n"
      "s_mov_b32 s87, 0x319\n"
      "s_mov_b32 s88, 0x120339\n"
      "s_mov_b32 s89, 0x2003b20\n"
      "s_mov_b32 s62, 816\n"
      "s_mov_b32 s64, 0\n"
      "s_branch .Ldescdone_%=\n"
      ".Ldesc1_%=:\n"
      "s_mov_b32 s70, 0xa48e88\n"
      "s_mov_b32 s71, 0x8f09\n"
      "s_mov_b32 s72, 0x408f2a\n"
      "s_mov_b32 s73, 0x8f4b\n"
      "s_mov_b32 s74, 0x8f6c\n"
      "s_mov_b32 s75, 0x8f8d\n"
      "s_mov_b32 s76, 0x8fae\n"
      "s_mov_b32 s77, 0x8fcf\n"
      "s_mov_b32 s78, 0x1008ff0\n"
      "s_mov_b32 s79, 0x9011\n"
      "s_mov_b32 s80, 0x9032\n"
      "s_mov_b32 s81, 0x9053\n"
      "s_mov_b32 s82, 0x9074\n"
      "s_mov_b32 s83, 0x9095\n"
      "s_mov_b32 s84, 0x90b6\n"
      "s_mov_b32 s85, 0x90d7\n"
      "s_mov_b32 s86, 0x90f8\n"
      "s_mov_b32 s87, 0x9119\n"
      "s_mov_b32 s88, 0x129139\n"
      "s_mov_b32 s89, 0x2004920\n"
      "s_mov_b32 s62, 1904\n"
      "s_mov_b32 s64, 1088\n"
      "s_branch .Ldescdone_%=\n"
      ".Ldesc2_%=:\n"
      "s_mov_b32 s70, 0x200085\n"
      "s_mov_b32 s71, 0x9100ab\n"
      "s_mov_b32 s72, 0x451d0c\n"
      "s_mov_b32 s73, 0x11d8d\n"
      "s_mov_b32 s74, 0x11dae\n"
      "s_mov_b32 s75, 0x11dcf\n"
      "s_mov_b32 s76, 0x1011df0\n"
      "s_mov_b32 s77, 0x11e11\n"
      "s_mov_b32 s78, 0x11e32\n"
      "s_mov_b32 s79, 0x11e53\n"
      "s_mov_b32 s80, 0x11e74\n"
      "s_mov_b32 s81, 0x11e95\n"
      "s_mov_b32 s82, 0x11eb6\n"
      "s_mov_b32 s83, 0x11ed7\n"
      "s_mov_b32 s84, 0x11ef8\n"
      "s_mov_b32 s85, 0x11f19\n"
      "s_mov_b32 s86, 0x129f37\n"
      "s_mov_b32 s87, 0x6c898\n"
      "s_mov_b32 s88, 0x2004900\n"
      "s_mov_b32 s62, 1088\n"
      "s_mov_b32 s64, 0\n"
      "s_branch .Ldescdone_%=\n"
      ".Ldesc3_%=:\n"
      "s_mov_b32 s70, 0x2000c7\n"
      "s_mov_b32 s71, 0x9180ef\n"
      "s_mov_b32 s72, 0x145ab90\n"
      "s_mov_b32 s73, 0x1ac11\n"
      "s_mov_b32 s74, 0x1ac32\n"
      "s_mov_b32 s75, 0x1ac53\n"
      "s_mov_b32 s76, 0x1ac74\n"
      "s_mov_b32 s77, 0x1ac95\n"
      "s_mov_b32 s78, 0x1acb6\n"
      "s_mov_b32 s79, 0x1acd7\n"
      "s_mov_b32 s80, 0x1acf8\n"
      "s_mov_b32 s81, 0x1ad19\n"
      "s_mov_b32 s82, 0x122d33\n"
      "s_mov_b32 s83, 0x63a14\n"
      "s_mov_b32 s84, 0x23a95\n"
      "s_mov_b32 s85, 0x23ab6\n"
      "s_mov_b32 s86, 0x23ad7\n"
      "s_mov_b32 s87, 0x23af8\n"
      "s_mov_b32 s88, 0x133b19\n"
      "s_mov_b32 s89, 0x2085720\n"
      "s_mov_b32 s62, 1632\n"
      "s_mov_b32 s64, 0\n"
      ".Ldescdone_%=:\n"
      "s_lshr_b32 s52, s50, 1\n"
      "s_xor_b32 s52, s52, s50\n"
      "s_mul_i32 s52, s52, 1152\n"
      "s_add_u32 s52, s52, 113152\n"
      "v_mul_u32_u24 v242, 288, v249\n"
      "v_add_u32 v242, v250, v242\n"
      "v_add_u32 v242, s52, v242\n"
      "s_mov_b32 s68, 0x44800000\n"
      "v_lshlrev_b32 v243, 5, v248\n"
      "v_add_u32 v246, 160256, v250\n"
      "v_add_u32 v243, 160768, v243\n"
      "s_waitcnt vmcnt(13)\n"
      "v_lshl_add_u32 v112, v112, 8, v250\n"
      "v_lshl_add_u32 v113, v113, 8, v250\n"
      "global_load_dwordx4 v[80:83], v112, s[38:39]\n"
      "v_lshl_add_u32 v114, v114, 8, v250\n"
      "global_load_dwordx4 v[84:87], v113, s[38:39]\n"
      "v_lshl_add_u32 v115, v115, 8, v250\n"
      "global_load_dwordx4 v[88:91], v114, s[38:39]\n"
      "v_lshl_add_u32 v116, v116, 8, v250\n"
      "global_load_dwordx4 v[92:95], v115, s[38:39]\n"
      "v_lshl_add_u32 v117, v117, 8, v250\n"
      "global_load_dwordx4 v[96:99], v116, s[38:39]\n"
      "v_lshl_add_u32 v118, v118, 8, v250\n"
      "global_load_dwordx4 v[100:103], v117, s[38:39]\n"
      "v_lshl_add_u32 v119, v119, 8, v250\n"
      "global_load_dwordx4 v[104:107], v118, s[38:39]\n"
      "v_lshl_add_u32 v120, v120, 8, v250\n"
      "global_load_dwordx4 v[108:111], v119, s[38:39]\n"
      "s_waitcnt vmcnt(18)\n"
      "v_lshlrev_b32 v40, 16, v64\n"
      "v_and_b32 v41, s69, v64\n"
      "v_lshlrev_b32 v42, 16, v65\n"
      "v_and_b32 v43, s69, v65\n"
      "v_lshlrev_b32 v44, 16, v66\n"
      "v_and_b32 v45, s69, v66\n"
      "v_lshlrev_b32 v46, 16, v67\n"
      "v_and_b32 v47, s69, v67\n"
      "v_lshlrev_b32 v48, 16, v56\n"
      "v_lshlrev_b32 v32, 16, v60\n"
      "v_and_b32 v49, s69, v56\n"
      "v_and_b32 v33, s69, v60\n"
      "v_lshlrev_b32 v50, 16, v57\n"
      "v_lshlrev_b32 v34, 16, v61\n"
      "v_and_b32 v51, s69, v57\n"
      "v_and_b32 v35, s69, v61\n"
      "v_lshlrev_b32 v52, 16, v58\n"
      "v_lshlrev_b32 v36, 16, v62\n"
      "v_and_b32 v53, s69, v58\n"
      "v_and_b32 v37, s69, v62\n"
      "v_lshlrev_b32 v54, 16, v59\n"
      "v_lshlrev_b32 v38, 16, v63\n"
      "v_and_b32 v55, s69, v59\n"
      "v_and_b32 v39, s69, v63\n"
      "v_max3_f32 v245, |v40|, |v41|, |v42|\n"
      "ds_write_b128 v243, v[48:51]\n"
      "v_max3_f32 v245, |v43|, |v44|, v245\n"
      "ds_write_b128 v243, v[52:55] offset:16\n"
      "v_max3_f32 v245, |v45|, |v46|, v245\n"
      "ds_write_b128 v243, v[32:35] offset:512\n"
      "v_max_f32_e64 v245, |v47|, v245\n"
      "ds_write_b128 v243, v[36:39] offset:528\n"
      "v_lshl_add_u32 v121, v121, 8, v250\n"
      "v_lshl_add_u32 v122, v122, 8, v250\n"
      "v_max_f32_dpp v245, v245, v245 row_ror:8 row_mask:0xf bank_mask:0xf\n"
      "v_lshl_add_u32 v123, v123, 8, v250\n"
      "v_lshl_add_u32 v124, v124, 8, v250\n"
      "v_lshl_add_u32 v125, v125, 8, v250\n"
      "v_lshl_add_u32 v126, v126, 8, v250\n"
      "v_max_f32_dpp v245, v245, v245 row_ror:4 row_mask:0xf bank_mask:0xf\n"
      "v_lshl_add_u32 v127, v127, 8, v250\n"
      "v_lshl_add_u32 v192, v128, 8, v250\n"
      "v_lshl_add_u32 v193, v129, 8, v250\n"
      "v_lshl_add_u32 v194, v130, 8, v250\n"
      "v_max_f32_dpp v245, v245, v245 row_ror:2 row_mask:0xf bank_mask:0xf\n"
      "v_lshl_add_u32 v195, v131, 8, v250\n"
      "v_lshl_add_u32 v196, v132, 8, v250\n"
      "v_lshl_add_u32 v197, v133, 8, v250\n"
      "v_lshl_add_u32 v198, v134, 8, v250\n"
      "v_max_f32_dpp v245, v245, v245 row_ror:1 row_mask:0xf bank_mask:0xf\n"
      "v_lshl_add_u32 v199, v135, 8, v250\n"
      "v_lshl_add_u32 v254, v136, 8, v250\n"
      "v_lshl_add_u32 v255, v137, 8, v250\n"
      "v_readfirstlane_b32 s57, v245\n"
      "v_accvgpr_write_b32 a128, v192\n"
      "v_accvgpr_write_b32 a132, v193\n"
      "v_accvgpr_write_b32 a136, v194\n"
      "v_accvgpr_write_b32 a140, v195\n"
      "v_accvgpr_write_b32 a144, v196\n"
      "v_accvgpr_write_b32 a148, v197\n"
      "v_accvgpr_write_b32 a152, v198\n"
      "v_accvgpr_write_b32 a156, v199\n"
      "v_accvgpr_write_b32 a160, v254\n"
      "v_accvgpr_write_b32 a164, v255\n"
      "v_mov_b32 v76, 0\n"
      "s_lshr_b32 s57, s57, 23\n"
      "s_sub_u32 s52, s57, 67\n"
      "s_cmp_gt_u32 s52, 120\n"
      "s_cselect_b32 s52, s57, 0\n"
      "s_or_b32 s51, s51, s52\n"
      "s_max_u32 s57, s57, 67\n"
      "s_min_u32 s57, s57, 187\n"
      "s_sub_u32 s54, 260, s57\n"
      "s_sub_u32 s67, s57, 6\n"
      "s_lshl_b32 s54, s54, 23\n"
      "s_lshl_b32 s67, s67, 23\n"
      "v_mul_f32 v40, s54, v40\n"
      "v_mul_f32 v41, s54, v41\n"
      "v_mul_f32 v42, s54, v42\n"
      "v_mul_f32 v43, s54, v43\n"
      "v_mul_f32 v44, s54, v44\n"
      "v_mul_f32 v45, s54, v45\n"
      "v_mul_f32 v46, s54, v46\n"
      "v_mul_f32 v47, s54, v47\n"
      "v_cvt_pk_f16_f32 v64, v40, v41\n"
      "v_cvt_pk_f16_f32 v65, v42, v43\n"
      "v_cvt_pk_f16_f32 v66, v44, v45\n"
      "v_cvt_pk_f16_f32 v67, v46, v47\n"
      "s_mov_b32 s94, 0x44800000\n"
      "s_mov_b32 s95, 0x44800000\n"
      "ds_write_b128 v246, v[64:67]\n"
      "s_waitcnt vmcnt(10)\n"
      "v_lshlrev_b32 v32, 16, v0\n"
      "v_and_b32 v33, s69, v0\n"
      "v_lshlrev_b32 v34, 16, v1\n"
      "v_and_b32 v35, s69, v1\n"
      "v_lshlrev_b32 v36, 16, v2\n"
      "v_and_b32 v37, s69, v2\n"
      "v_lshlrev_b32 v38, 16, v3\n"
      "v_and_b32 v39, s69, v3\n"
      "v_mul_f32 v68, v32, v48\n"
      "v_max3_f32 v76, |v32|, |v33|, v76\n"
      "v_fmac_f32 v68, v33, v49\n"
      "v_fmac_f32 v68, v34, v50\n"
      "v_fmac_f32 v68, v35, v51\n"
      "v_fmac_f32 v68, v36, v52\n"
      "v_fmac_f32 v68, v37, v53\n"
      "v_fmac_f32 v68, v38, v54\n"
      "v_fmac_f32 v68, v39, v55\n"
      "v_pk_mul_f32 v[32:33], v[32:33], s[94:95]\n"
      "v_pk_mul_f32 v[34:35], v[34:35], s[94:95]\n"
      "v_pk_mul_f32 v[36:37], v[36:37], s[94:95]\n"
      "v_pk_mul_f32 v[38:39], v[38:39], s[94:95]\n"
      "v_cvt_pk_f16_f32 v0, v32, v33\n"
      "v_cvt_pk_f16_f32 v1, v34, v35\n"
      "v_cvt_pk_f16_f32 v2, v36, v37\n"
      "v_cvt_pk_f16_f32 v3, v38, v39\n"
      "ds_write_b128 v242, v[0:3] offset:0\n"
      "v_lshlrev_b32 v40, 16, v4\n"
      "v_and_b32 v41, s69, v4\n"
      "v_lshlrev_b32 v42, 16, v5\n"
      "v_and_b32 v43, s69, v5\n"
      "v_lshlrev_b32 v44, 16, v6\n"
      "v_and_b32 v45, s69, v6\n"
      "v_lshlrev_b32 v46, 16, v7\n"
      "v_and_b32 v47, s69, v7\n"
      "v_mul_f32 v69, v40, v48\n"
      "v_max3_f32 v76, |v40|, |v41|, v76\n"
      "v_fmac_f32 v69, v41, v49\n"
      "v_fmac_f32 v69, v42, v50\n"
      "v_fmac_f32 v69, v43, v51\n"
      "v_fmac_f32 v69, v44, v52\n"
      "v_fmac_f32 v69, v45, v53\n"
      "v_fmac_f32 v69, v46, v54\n"
      "v_fmac_f32 v69, v47, v55\n"
      "v_pk_mul_f32 v[40:41], v[40:41], s[94:95]\n"
      "v_pk_mul_f32 v[42:43], v[42:43], s[94:95]\n"
      "v_pk_mul_f32 v[44:45], v[44:45], s[94:95]\n"
      "v_pk_mul_f32 v[46:47], v[46:47], s[94:95]\n"
      "v_cvt_pk_f16_f32 v4, v40, v41\n"
      "v_cvt_pk_f16_f32 v5, v42, v43\n"
      "v_cvt_pk_f16_f32 v6, v44, v45\n"
      "v_cvt_pk_f16_f32 v7, v46, v47\n"
      "ds_write_b128 v242, v[4:7] offset:4608\n"
      "v_lshlrev_b32 v32, 16, v8\n"
      "v_and_b32 v33, s69, v8\n"
      "v_lshlrev_b32 v34, 16, v9\n"
      "v_and_b32 v35, s69, v9\n"
      "v_lshlrev_b32 v36, 16, v10\n"
      "v_and_b32 v37, s69, v10\n"
      "v_lshlrev_b32 v38, 16, v11\n"
      "v_and_b32 v39, s69, v11\n"
      "v_mul_f32 v70, v32, v48\n"
      "v_max3_f32 v76, |v32|, |v33|, v76\n"
      "v_fmac_f32 v70, v33, v49\n"
      "v_fmac_f32 v70, v34, v50\n"
      "v_fmac_f32 v70, v35, v51\n"
      "v_fmac_f32 v70, v36, v52\n"
      "v_fmac_f32 v70, v37, v53\n"
      "v_fmac_f32 v70, v38, v54\n"
      "v_fmac_f32 v70, v39, v55\n"
      "v_pk_mul_f32 v[32:33], v[32:33], s[94:95]\n"
      "v_pk_mul_f32 v[34:35], v[34:35], s[94:95]\n"
      "v_pk_mul_f32 v[36:37], v[36:37], s[94:95]\n"
      "v_pk_mul_f32 v[38:39], v[38:39], s[94:95]\n"
      "v_cvt_pk_f16_f32 v8, v32, v33\n"
      "v_cvt_pk_f16_f32 v9, v34, v35\n"
      "v_cvt_pk_f16_f32 v10, v36, v37\n"
      "v_cvt_pk_f16_f32 v11, v38, v39\n"
      "ds_write_b128 v242, v[8:11] offset:9216\n"
      "v_lshlrev_b32 v40, 16, v12\n"
      "v_and_b32 v41, s69, v12\n"
      "v_lshlrev_b32 v42, 16, v13\n"
      "v_and_b32 v43, s69, v13\n"
      "v_lshlrev_b32 v44, 16, v14\n"
      "v_and_b32 v45, s69, v14\n"
      "v_lshlrev_b32 v46, 16, v15\n"
      "v_and_b32 v47, s69, v15\n"
      "v_mul_f32 v71, v40, v48\n"
      "v_max3_f32 v76, |v40|, |v41|, v76\n"
      "v_fmac_f32 v71, v41, v49\n"
      "v_fmac_f32 v71, v42, v50\n"
      "v_fmac_f32 v71, v43, v51\n"
      "v_fmac_f32 v71, v44, v52\n"
      "v_fmac_f32 v71, v45, v53\n"
      "v_fmac_f32 v71, v46, v54\n"
      "v_fmac_f32 v71, v47, v55\n"
      "v_pk_mul_f32 v[40:41], v[40:41], s[94:95]\n"
      "v_pk_mul_f32 v[42:43], v[42:43], s[94:95]\n"
      "v_pk_mul_f32 v[44:45], v[44:45], s[94:95]\n"
      "v_pk_mul_f32 v[46:47], v[46:47], s[94:95]\n"
      "v_cvt_pk_f16_f32 v12, v40, v41\n"
      "v_cvt_pk_f16_f32 v13, v42, v43\n"
      "v_cvt_pk_f16_f32 v14, v44, v45\n"
      "v_cvt_pk_f16_f32 v15, v46, v47\n"
      "ds_write_b128 v242, v[12:15] offset:13824\n"
      "v_lshlrev_b32 v32, 16, v16\n"
      "v_and_b32 v33, s69, v16\n"
      "v_lshlrev_b32 v34, 16, v17\n"
      "v_and_b32 v35, s69, v17\n"
      "v_lshlrev_b32 v36, 16, v18\n"
      "v_and_b32 v37, s69, v18\n"
      "v_lshlrev_b32 v38, 16, v19\n"
      "v_and_b32 v39, s69, v19\n"
      "v_mul_f32 v72, v32, v48\n"
      "v_max3_f32 v76, |v32|, |v33|, v76\n"
      "v_fmac_f32 v72, v33, v49\n"
      "v_fmac_f32 v72, v34, v50\n"
      "v_fmac_f32 v72, v35, v51\n"
      "v_fmac_f32 v72, v36, v52\n"
      "v_fmac_f32 v72, v37, v53\n"
      "v_fmac_f32 v72, v38, v54\n"
      "v_fmac_f32 v72, v39, v55\n"
      "v_pk_mul_f32 v[32:33], v[32:33], s[94:95]\n"
      "v_pk_mul_f32 v[34:35], v[34:35], s[94:95]\n"
      "v_pk_mul_f32 v[36:37], v[36:37], s[94:95]\n"
      "v_pk_mul_f32 v[38:39], v[38:39], s[94:95]\n"
      "v_cvt_pk_f16_f32 v16, v32, v33\n"
      "v_cvt_pk_f16_f32 v17, v34, v35\n"
      "v_cvt_pk_f16_f32 v18, v36, v37\n"
      "v_cvt_pk_f16_f32 v19, v38, v39\n"
      "ds_write_b128 v242, v[16:19] offset:18432\n"
      "v_lshlrev_b32 v40, 16, v20\n"
      "v_and_b32 v41, s69, v20\n"
      "v_lshlrev_b32 v42, 16, v21\n"
      "v_and_b32 v43, s69, v21\n"
      "v_lshlrev_b32 v44, 16, v22\n"
      "v_and_b32 v45, s69, v22\n"
      "v_lshlrev_b32 v46, 16, v23\n"
      "v_and_b32 v47, s69, v23\n"
      "v_mul_f32 v73, v40, v48\n"
      "v_max3_f32 v76, |v40|, |v41|, v76\n"
      "v_fmac_f32 v73, v41, v49\n"
      "v_fmac_f32 v73, v42, v50\n"
      "v_fmac_f32 v73, v43, v51\n"
      "v_fmac_f32 v73, v44, v52\n"
      "v_fmac_f32 v73, v45, v53\n"
      "v_fmac_f32 v73, v46, v54\n"
      "v_fmac_f32 v73, v47, v55\n"
      "v_pk_mul_f32 v[40:41], v[40:41], s[94:95]\n"
      "v_pk_mul_f32 v[42:43], v[42:43], s[94:95]\n"
      "v_pk_mul_f32 v[44:45], v[44:45], s[94:95]\n"
      "v_pk_mul_f32 v[46:47], v[46:47], s[94:95]\n"
      "v_cvt_pk_f16_f32 v20, v40, v41\n"
      "v_cvt_pk_f16_f32 v21, v42, v43\n"
      "v_cvt_pk_f16_f32 v22, v44, v45\n"
      "v_cvt_pk_f16_f32 v23, v46, v47\n"
      "ds_write_b128 v242, v[20:23] offset:23040\n"
      "v_lshlrev_b32 v32, 16, v24\n"
      "v_and_b32 v33, s69, v24\n"
      "v_lshlrev_b32 v34, 16, v25\n"
      "v_and_b32 v35, s69, v25\n"
      "v_lshlrev_b32 v36, 16, v26\n"
      "v_and_b32 v37, s69, v26\n"
      "v_lshlrev_b32 v38, 16, v27\n"
      "v_and_b32 v39, s69, v27\n"
      "v_mul_f32 v74, v32, v48\n"
      "v_max3_f32 v76, |v32|, |v33|, v76\n"
      "v_fmac_f32 v74, v33, v49\n"
      "v_fmac_f32 v74, v34, v50\n"
      "v_fmac_f32 v74, v35, v51\n"
      "v_fmac_f32 v74, v36, v52\n"
      "v_fmac_f32 v74, v37, v53\n"
      "v_fmac_f32 v74, v38, v54\n"
      "v_fmac_f32 v74, v39, v55\n"
      "v_pk_mul_f32 v[32:33], v[32:33], s[94:95]\n"
      "v_pk_mul_f32 v[34:35], v[34:35], s[94:95]\n"
      "v_pk_mul_f32 v[36:37], v[36:37], s[94:95]\n"
      "v_pk_mul_f32 v[38:39], v[38:39], s[94:95]\n"
      "v_cvt_pk_f16_f32 v24, v32, v33\n"
      "v_cvt_pk_f16_f32 v25, v34, v35\n"
      "v_cvt_pk_f16_f32 v26, v36, v37\n"
      "v_cvt_pk_f16_f32 v27, v38, v39\n"
      "ds_write_b128 v242, v[24:27] offset:27648\n"
      "v_lshlrev_b32 v40, 16, v28\n"
      "v_and_b32 v41, s69, v28\n"
      "v_lshlrev_b32 v42, 16, v29\n"
      "v_and_b32 v43, s69, v29\n"
      "v_lshlrev_b32 v44, 16, v30\n"
      "v_and_b32 v45, s69, v30\n"
      "v_lshlrev_b32 v46, 16, v31\n"
      "v_and_b32 v47, s69, v31\n"
      "v_mul_f32 v75, v40, v48\n"
      "v_max3_f32 v76, |v40|, |v41|, v76\n"
      "v_fmac_f32 v75, v41, v49\n"
      "v_fmac_f32 v75, v42, v50\n"
      "v_fmac_f32 v75, v43, v51\n"
      "v_fmac_f32 v75, v44, v52\n"
      "v_fmac_f32 v75, v45, v53\n"
      "v_fmac_f32 v75, v46, v54\n"
      "v_fmac_f32 v75, v47, v55\n"
      "v_pk_mul_f32 v[40:41], v[40:41], s[94:95]\n"
      "v_pk_mul_f32 v[42:43], v[42:43], s[94:95]\n"
      "v_pk_mul_f32 v[44:45], v[44:45], s[94:95]\n"
      "v_pk_mul_f32 v[46:47], v[46:47], s[94:95]\n"
      "v_cvt_pk_f16_f32 v28, v40, v41\n"
      "v_cvt_pk_f16_f32 v29, v42, v43\n"
      "v_cvt_pk_f16_f32 v30, v44, v45\n"
      "v_cvt_pk_f16_f32 v31, v46, v47\n"
      "ds_write_b128 v242, v[28:31] offset:32256\n"
      "v_cmp_le_f32 vcc, 0x39800000, v76\n"
      "v_add_f32_dpp v68, v68, v68 row_ror:8 row_mask:0xf bank_mask:0xf\n"
      "v_add_f32_dpp v69, v69, v69 row_ror:8 row_mask:0xf bank_mask:0xf\n"
      "v_add_f32_dpp v70, v70, v70 row_ror:8 row_mask:0xf bank_mask:0xf\n"
      "v_add_f32_dpp v71, v71, v71 row_ror:8 row_mask:0xf bank_mask:0xf\n"
      "v_add_f32_dpp v72, v72, v72 row_ror:8 row_mask:0xf bank_mask:0xf\n"
      "v_add_f32_dpp v73, v73, v73 row_ror:8 row_mask:0xf bank_mask:0xf\n"
      "v_add_f32_dpp v74, v74, v74 row_ror:8 row_mask:0xf bank_mask:0xf\n"
      "v_add_f32_dpp v75, v75, v75 row_ror:8 row_mask:0xf bank_mask:0xf\n"
      "v_add_f32_dpp v68, v68, v68 row_ror:4 row_mask:0xf bank_mask:0xf\n"
      "v_add_f32_dpp v69, v69, v69 row_ror:4 row_mask:0xf bank_mask:0xf\n"
      "v_add_f32_dpp v70, v70, v70 row_ror:4 row_mask:0xf bank_mask:0xf\n"
      "v_add_f32_dpp v71, v71, v71 row_ror:4 row_mask:0xf bank_mask:0xf\n"
      "v_add_f32_dpp v72, v72, v72 row_ror:4 row_mask:0xf bank_mask:0xf\n"
      "v_add_f32_dpp v73, v73, v73 row_ror:4 row_mask:0xf bank_mask:0xf\n"
      "v_add_f32_dpp v74, v74, v74 row_ror:4 row_mask:0xf bank_mask:0xf\n"
      "v_add_f32_dpp v75, v75, v75 row_ror:4 row_mask:0xf bank_mask:0xf\n"
      "v_add_f32_dpp v68, v68, v68 row_ror:2 row_mask:0xf bank_mask:0xf\n"
      "v_add_f32_dpp v69, v69, v69 row_ror:2 row_mask:0xf bank_mask:0xf\n"
      "v_add_f32_dpp v70, v70, v70 row_ror:2 row_mask:0xf bank_mask:0xf\n"
      "v_add_f32_dpp v71, v71, v71 row_ror:2 row_mask:0xf bank_mask:0xf\n"
      "v_add_f32_dpp v72, v72, v72 row_ror:2 row_mask:0xf bank_mask:0xf\n"
      "v_add_f32_dpp v73, v73, v73 row_ror:2 row_mask:0xf bank_mask:0xf\n"
      "v_add_f32_dpp v74, v74, v74 row_ror:2 row_mask:0xf bank_mask:0xf\n"
      "v_add_f32_dpp v75, v75, v75 row_ror:2 row_mask:0xf bank_mask:0xf\n"
      "v_add_f32_dpp v68, v68, v68 row_ror:1 row_mask:0xf bank_mask:0xf\n"
      "v_add_f32_dpp v69, v69, v69 row_ror:1 row_mask:0xf bank_mask:0xf\n"
      "v_add_f32_dpp v70, v70, v70 row_ror:1 row_mask:0xf bank_mask:0xf\n"
      "v_add_f32_dpp v71, v71, v71 row_ror:1 row_mask:0xf bank_mask:0xf\n"
      "v_add_f32_dpp v72, v72, v72 row_ror:1 row_mask:0xf bank_mask:0xf\n"
      "v_add_f32_dpp v73, v73, v73 row_ror:1 row_mask:0xf bank_mask:0xf\n"
      "v_add_f32_dpp v74, v74, v74 row_ror:1 row_mask:0xf bank_mask:0xf\n"
      "v_add_f32_dpp v75, v75, v75 row_ror:1 row_mask:0xf bank_mask:0xf\n"
      "s_cmp_eq_u64 vcc, 0\n"
      "s_cselect_b32 s52, 1, 0\n"
      "s_or_b32 s51, s51, s52\n"
      "v_max3_f32 v76, |v68|, |v69|, |v70|\n"
      "s_lshl_b32 s52, s50, 4\n"
      "v_max3_f32 v77, |v71|, |v72|, |v73|\n"
      "s_add_u32 s52, s52, 160576\n"
      "v_lshlrev_b32 v243, 2, v249\n"
      "v_max3_f32 v76, |v74|, |v75|, v76\n"
      "v_add_u32 v243, s52, v243\n"
      "v_max_f32 v76, v76, v77\n"
      "ds_write_b32 v243, v76\n"
      "v_lshlrev_b32 v244, 2, v248\n"
      "v_lshlrev_b32 v243, 4, v249\n"
      "v_add_u32 v244, 160576, v244\n"
      "v_add_u32 v243, 160768, v243\n"
      "s_waitcnt lgkmcnt(0)\n"
      "s_barrier\n"
      "ds_read_b32 v76, v244\n"
      "ds_read_b128 v[0:3], v243 offset:512\n"
      "ds_read_b128 v[4:7], v243 offset:576\n"
      "ds_read_b128 v[8:11], v243 offset:640\n"
      "ds_read_b128 v[12:15], v243 offset:704\n"
      "ds_read_b128 v[16:19], v243 offset:768\n"
      "ds_read_b128 v[20:23], v243 offset:832\n"
      "ds_read_b128 v[24:27], v243 offset:896\n"
      "ds_read_b128 v[28:31], v243 offset:960\n"
      "s_waitcnt lgkmcnt(8)\n"
      "v_max_f32_dpp v76, v76, v76 row_ror:8 row_mask:0xf bank_mask:0xf\n"
      "ds_read_b128 v[32:35], v243 offset:0\n"
      "ds_read_b128 v[36:39], v243 offset:64\n"
      "v_max_f32_dpp v76, v76, v76 row_ror:4 row_mask:0xf bank_mask:0xf\n"
      "ds_read_b128 v[40:43], v243 offset:128\n"
      "ds_read_b128 v[44:47], v243 offset:192\n"
      "v_max_f32_dpp v76, v76, v76 row_ror:2 row_mask:0xf bank_mask:0xf\n"
      "ds_read_b128 v[48:51], v243 offset:256\n"
      "ds_read_b128 v[52:55], v243 offset:320\n"
      "v_max_f32_dpp v76, v76, v76 row_ror:1 row_mask:0xf bank_mask:0xf\n"
      "ds_read_b128 v[56:59], v243 offset:384\n"
      "ds_read_b128 v[60:63], v243 offset:448\n"
      "ds_read_b64_tr_b16 a[0:1], v78 offset:0\n"
      "v_readfirstlane_b32 s58, v76\n"
      "ds_read_b64_tr_b16 a[2:3], v79 offset:0\n"
      "ds_read_b64_tr_b16 a[4:5], v78 offset:9216\n"
      "ds_read_b64_tr_b16 a[6:7], v79 offset:9216\n"
      "ds_read_b64_tr_b16 a[8:9], v78 offset:18432\n"
      "ds_read_b64_tr_b16 a[10:11], v79 offset:18432\n"
      "ds_read_b64_tr_b16 a[12:13], v78 offset:27648\n"
      "ds_read_b64_tr_b16 a[14:15], v79 offset:27648\n"
      "s_lshr_b32 s58, s58, 23\n"
      "s_sub_u32 s52, s58, 67\n"
      "s_cmp_gt_u32 s52, 120\n"
      "s_cselect_b32 s52, s58, 0\n"
      "s_or_b32 s51, s51, s52\n"
      "s_max_u32 s58, s58, 67\n"
      "s_min_u32 s58, s58, 187\n"
      "s_sub_u32 s53, 260, s58\n"
      "s_sub_u32 s96, 250, s58\n"
      "s_sub_u32 s66, s58, 6\n"
      "s_lshl_b32 s53, s53, 23\n"
      "s_lshl_b32 s96, s96, 23\n"
      "s_lshl_b32 s66, s66, 23\n"
      "s_mov_b32 s97, s96\n"
      "ds_read_b64_tr_b16 a[16:17], v78 offset:32\n"
      "ds_read_b64_tr_b16 a[18:19], v79 offset:32\n"
      "ds_read_b64_tr_b16 a[20:21], v78 offset:9248\n"
      "ds_read_b64_tr_b16 a[22:23], v79 offset:9248\n"
      "ds_read_b64_tr_b16 a[24:25], v78 offset:18464\n"
      "ds_read_b64_tr_b16 a[26:27], v79 offset:18464\n"
      "ds_read_b64_tr_b16 a[28:29], v78 offset:27680\n"
      "ds_read_b64_tr_b16 a[30:31], v79 offset:27680\n"
      "ds_read_b64_tr_b16 a[32:33], v78 offset:64\n"
      "ds_read_b64_tr_b16 a[34:35], v79 offset:64\n"
      "ds_read_b64_tr_b16 a[36:37], v78 offset:9280\n"
      "ds_read_b64_tr_b16 a[38:39], v79 offset:9280\n"
      "ds_read_b64_tr_b16 a[40:41], v78 offset:18496\n"
      "ds_read_b64_tr_b16 a[42:43], v79 offset:18496\n"
      "ds_read_b64_tr_b16 a[44:45], v78 offset:27712\n"
      "ds_read_b64_tr_b16 a[46:47], v79 offset:27712\n"
      "s_lshl_b32 s52, s50, 3\n"
      "s_add_u32 s52, s52, 160000\n"
      "v_lshlrev_b32 v243, 1, v249\n"
      "v_add_u32 v243, s52, v243\n"
      "ds_read_b64_tr_b16 a[48:49], v78 offset:96\n"
      "v_mul_f32 v68, s53, v68\n"
      "ds_read_b64_tr_b16 a[50:51], v79 offset:96\n"
      "v_mul_f32 v69, s53, v69\n"
      "ds_read_b64_tr_b16 a[52:53], v78 offset:9312\n"
      "v_mul_f32 v70, s53, v70\n"
      "ds_read_b64_tr_b16 a[54:55], v79 offset:9312\n"
      "v_mul_f32 v71, s53, v71\n"
      "ds_read_b64_tr_b16 a[56:57], v78 offset:18528\n"
      "v_mul_f32 v72, s53, v72\n"
      "ds_read_b64_tr_b16 a[58:59], v79 offset:18528\n"
      "v_mul_f32 v73, s53, v73\n"
      "ds_read_b64_tr_b16 a[60:61], v78 offset:27744\n"
      "v_mul_f32 v74, s53, v74\n"
      "ds_read_b64_tr_b16 a[62:63], v79 offset:27744\n"
      "v_mul_f32 v75, s53, v75\n"
      "ds_read_b64_tr_b16 a[64:65], v78 offset:128\n"
      "v_cvt_f16_f32 v68, v68\n"
      "ds_read_b64_tr_b16 a[66:67], v79 offset:128\n"
      "v_cvt_f16_f32 v69, v69\n"
      "ds_read_b64_tr_b16 a[68:69], v78 offset:9344\n"
      "v_cvt_f16_f32 v70, v70\n"
      "ds_read_b64_tr_b16 a[70:71], v79 offset:9344\n"
      "v_cvt_f16_f32 v71, v71\n"
      "ds_read_b64_tr_b16 a[72:73], v78 offset:18560\n"
      "v_cvt_f16_f32 v72, v72\n"
      "ds_read_b64_tr_b16 a[74:75], v79 offset:18560\n"
      "v_cvt_f16_f32 v73, v73\n"
      "ds_read_b64_tr_b16 a[76:77], v78 offset:27776\n"
      "v_cvt_f16_f32 v74, v74\n"
      "ds_read_b64_tr_b16 a[78:79], v79 offset:27776\n"
      "v_cvt_f16_f32 v75, v75\n"
      "ds_read_b64_tr_b16 a[80:81], v78 offset:160\n"
      "ds_write_b16 v243, v68 offset:0\n"
      "ds_read_b64_tr_b16 a[82:83], v79 offset:160\n"
      "ds_write_b16 v243, v69 offset:32\n"
      "ds_read_b64_tr_b16 a[84:85], v78 offset:9376\n"
      "ds_write_b16 v243, v70 offset:64\n"
      "ds_read_b64_tr_b16 a[86:87], v79 offset:9376\n"
      "ds_write_b16 v243, v71 offset:96\n"
      "ds_read_b64_tr_b16 a[88:89], v78 offset:18592\n"
      "ds_write_b16 v243, v72 offset:128\n"
      "ds_read_b64_tr_b16 a[90:91], v79 offset:18592\n"
      "ds_write_b16 v243, v73 offset:160\n"
      "ds_read_b64_tr_b16 a[92:93], v78 offset:27808\n"
      "ds_write_b16 v243, v74 offset:192\n"
      "ds_read_b64_tr_b16 a[94:95], v79 offset:27808\n"
      "ds_write_b16 v243, v75 offset:224\n"
      "ds_read_b64_tr_b16 a[96:97], v78 offset:192\n"
      "s_waitcnt lgkmcnt(14)\n"
      "v_pk_mul_f32 v[32:33], v[32:33], s[96:97]\n"
      "ds_read_b64_tr_b16 a[98:99], v79 offset:192\n"
      "v_pk_mul_f32 v[34:35], v[34:35], s[96:97]\n"
      "ds_read_b64_tr_b16 a[100:101], v78 offset:9408\n"
      "v_pk_mul_f32 v[36:37], v[36:37], s[96:97]\n"
      "ds_read_b64_tr_b16 a[102:103], v79 offset:9408\n"
      "v_pk_mul_f32 v[38:39], v[38:39], s[96:97]\n"
      "ds_read_b64_tr_b16 a[104:105], v78 offset:18624\n"
      "v_pk_mul_f32 v[40:41], v[40:41], s[96:97]\n"
      "ds_read_b64_tr_b16 a[106:107], v79 offset:18624\n"
      "v_pk_mul_f32 v[42:43], v[42:43], s[96:97]\n"
      "ds_read_b64_tr_b16 a[108:109], v78 offset:27840\n"
      "v_pk_mul_f32 v[44:45], v[44:45], s[96:97]\n"
      "ds_read_b64_tr_b16 a[110:111], v79 offset:27840\n"
      "v_pk_mul_f32 v[46:47], v[46:47], s[96:97]\n"
      "ds_read_b64_tr_b16 a[112:113], v78 offset:224\n"
      "v_pk_mul_f32 v[48:49], v[48:49], s[96:97]\n"
      "ds_read_b64_tr_b16 a[114:115], v79 offset:224\n"
      "v_pk_mul_f32 v[50:51], v[50:51], s[96:97]\n"
      "ds_read_b64_tr_b16 a[116:117], v78 offset:9440\n"
      "v_pk_mul_f32 v[52:53], v[52:53], s[96:97]\n"
      "ds_read_b64_tr_b16 a[118:119], v79 offset:9440\n"
      "v_pk_mul_f32 v[54:55], v[54:55], s[96:97]\n"
      "ds_read_b64_tr_b16 a[120:121], v78 offset:18656\n"
      "v_pk_mul_f32 v[56:57], v[56:57], s[96:97]\n"
      "ds_read_b64_tr_b16 a[122:123], v79 offset:18656\n"
      "v_pk_mul_f32 v[58:59], v[58:59], s[96:97]\n"
      "ds_read_b64_tr_b16 a[124:125], v78 offset:27872\n"
      "v_pk_mul_f32 v[60:61], v[60:61], s[96:97]\n"
      "ds_read_b64_tr_b16 a[126:127], v79 offset:27872\n"
      "v_pk_mul_f32 v[62:63], v[62:63], s[96:97]\n"
      "global_load_dwordx4 v[210:213], v120, s[38:39]\n"
      "global_load_dwordx4 v[214:217], v121, s[38:39]\n"
      "global_load_dwordx4 v[218:221], v122, s[38:39]\n"
      "global_load_dwordx4 v[222:225], v123, s[38:39]\n"
      "global_load_dwordx4 v[226:229], v124, s[38:39]\n"
      "global_load_dwordx4 v[230:233], v125, s[38:39]\n"
      "global_load_dwordx4 v[234:237], v126, s[38:39]\n"
      "global_load_dwordx4 v[238:241], v127, s[38:39]\n"
      "s_waitcnt vmcnt(12)\n"
      "v_mov_b32 v242, 0\n"
      "v_mov_b32 v244, 0\n"
      "v_lshlrev_b32 v144, 16, v80\n"
      "v_and_b32 v145, s69, v80\n"
      "v_lshlrev_b32 v146, 16, v81\n"
      "v_and_b32 v147, s69, v81\n"
      "v_max3_f32 v242, |v144|, |v145|, v242\n"
      "v_max3_f32 v244, |v146|, |v147|, v244\n"
      "v_lshlrev_b32 v148, 16, v82\n"
      "v_and_b32 v149, s69, v82\n"
      "v_lshlrev_b32 v150, 16, v83\n"
      "v_and_b32 v151, s69, v83\n"
      "v_max3_f32 v242, |v148|, |v149|, v242\n"
      "v_max3_f32 v244, |v150|, |v151|, v244\n"
      "v_lshlrev_b32 v152, 16, v84\n"
      "v_and_b32 v153, s69, v84\n"
      "v_lshlrev_b32 v154, 16, v85\n"
      "v_and_b32 v155, s69, v85\n"
      "v_max3_f32 v242, |v152|, |v153|, v242\n"
      "v_max3_f32 v244, |v154|, |v155|, v244\n"
      "v_lshlrev_b32 v156, 16, v86\n"
      "v_and_b32 v157, s69, v86\n"
      "v_lshlrev_b32 v158, 16, v87\n"
      "v_and_b32 v159, s69, v87\n"
      "v_max3_f32 v242, |v156|, |v157|, v242\n"
      "v_max3_f32 v244, |v158|, |v159|, v244\n"
      "v_lshlrev_b32 v160, 16, v88\n"
      "v_and_b32 v161, s69, v88\n"
      "v_lshlrev_b32 v162, 16, v89\n"
      "v_and_b32 v163, s69, v89\n"
      "v_max3_f32 v242, |v160|, |v161|, v242\n"
      "v_max3_f32 v244, |v162|, |v163|, v244\n"
      "v_lshlrev_b32 v164, 16, v90\n"
      "v_and_b32 v165, s69, v90\n"
      "v_lshlrev_b32 v166, 16, v91\n"
      "v_and_b32 v167, s69, v91\n"
      "v_max3_f32 v242, |v164|, |v165|, v242\n"
      "v_max3_f32 v244, |v166|, |v167|, v244\n"
      "v_lshlrev_b32 v168, 16, v92\n"
      "v_and_b32 v169, s69, v92\n"
      "v_lshlrev_b32 v170, 16, v93\n"
      "v_and_b32 v171, s69, v93\n"
      "v_max3_f32 v242, |v168|, |v169|, v242\n"
      "v_max3_f32 v244, |v170|, |v171|, v244\n"
      "v_lshlrev_b32 v172, 16, v94\n"
      "v_and_b32 v173, s69, v94\n"
      "v_lshlrev_b32 v174, 16, v95\n"
      "v_and_b32 v175, s69, v95\n"
      "v_max3_f32 v242, |v172|, |v173|, v242\n"
      "v_max3_f32 v244, |v174|, |v175|, v244\n"
      "v_max_f32 v242, v242, v244\n"
      "s_nop 1\n"
      "v_max_f32_dpp v242, v242, v242 row_ror:8 row_mask:0xf bank_mask:0xf\n"
      "s_nop 1\n"
      "v_max_f32_dpp v242, v242, v242 row_ror:4 row_mask:0xf bank_mask:0xf\n"
      "s_nop 1\n"
      "v_max_f32_dpp v242, v242, v242 row_ror:2 row_mask:0xf bank_mask:0xf\n"
      "s_nop 1\n"
      "v_max_f32_dpp v242, v242, v242 row_ror:1 row_mask:0xf bank_mask:0xf\n"
      "v_cmp_gt_u32 vcc, 0x00800000, v242\n"
      "s_lshl_b32 s52, s50, 4\n"
      "s_add_u32 s52, s52, 160512\n"
      "s_cmp_lg_u64 vcc, 0\n"
      "s_cbranch_scc1 .Lsigzero_%=\n"
      ".Lsig_%=:\n"
      "v_lshrrev_b32 v242, 23, v242\n"
      "v_max_u32 v243, 104, v242\n"
      "v_lshlrev_b32 v244, 2, v249\n"
      "v_min_u32 v243, 154, v243\n"
      "v_add_u32 v244, s52, v244\n"
      "v_cmp_ne_u32 vcc, v242, v243\n"
      "v_sub_u32 v208, 254, v243\n"
      "v_lshlrev_b32 v208, 23, v208\n"
      "s_or_b32 s51, s51, vcc_lo\n"
      "s_or_b32 s51, s51, vcc_hi\n"
      "v_mov_b32 v209, v208\n"
      "ds_write_b32 v244, v208\n"
      "v_pk_mul_f32 v[144:145], v[144:145], v[208:209]\n"
      "v_pk_mul_f32 v[146:147], v[146:147], v[208:209]\n"
      "v_pk_mul_f32 v[148:149], v[148:149], v[208:209]\n"
      "v_pk_mul_f32 v[150:151], v[150:151], v[208:209]\n"
      "v_cvt_pk_f16_f32 v80, v144, v145\n"
      "v_cvt_pk_f16_f32 v81, v146, v147\n"
      "v_cvt_pk_f16_f32 v82, v148, v149\n"
      "v_cvt_pk_f16_f32 v83, v150, v151\n"
      "ds_write_b128 v206, v[80:83] offset:0\n"
      "v_pk_mul_f32 v[152:153], v[152:153], v[208:209]\n"
      "v_pk_mul_f32 v[154:155], v[154:155], v[208:209]\n"
      "v_pk_mul_f32 v[156:157], v[156:157], v[208:209]\n"
      "v_pk_mul_f32 v[158:159], v[158:159], v[208:209]\n"
      "v_cvt_pk_f16_f32 v84, v152, v153\n"
      "v_cvt_pk_f16_f32 v85, v154, v155\n"
      "v_cvt_pk_f16_f32 v86, v156, v157\n"
      "v_cvt_pk_f16_f32 v87, v158, v159\n"
      "ds_write_b128 v206, v[84:87] offset:272\n"
      "v_pk_mul_f32 v[160:161], v[160:161], v[208:209]\n"
      "v_pk_mul_f32 v[162:163], v[162:163], v[208:209]\n"
      "v_pk_mul_f32 v[164:165], v[164:165], v[208:209]\n"
      "v_pk_mul_f32 v[166:167], v[166:167], v[208:209]\n"
      "v_cvt_pk_f16_f32 v88, v160, v161\n"
      "v_cvt_pk_f16_f32 v89, v162, v163\n"
      "v_cvt_pk_f16_f32 v90, v164, v165\n"
      "v_cvt_pk_f16_f32 v91, v166, v167\n"
      "ds_write_b128 v206, v[88:91] offset:544\n"
      "v_pk_mul_f32 v[168:169], v[168:169], v[208:209]\n"
      "v_pk_mul_f32 v[170:171], v[170:171], v[208:209]\n"
      "v_pk_mul_f32 v[172:173], v[172:173], v[208:209]\n"
      "v_pk_mul_f32 v[174:175], v[174:175], v[208:209]\n"
      "v_cvt_pk_f16_f32 v92, v168, v169\n"
      "v_cvt_pk_f16_f32 v93, v170, v171\n"
      "v_cvt_pk_f16_f32 v94, v172, v173\n"
      "v_cvt_pk_f16_f32 v95, v174, v175\n"
      "ds_write_b128 v206, v[92:95] offset:816\n"
      "s_waitcnt vmcnt(11)\n"
      "v_lshlrev_b32 v144, 16, v96\n"
      "v_and_b32 v145, s69, v96\n"
      "v_lshlrev_b32 v146, 16, v97\n"
      "v_and_b32 v147, s69, v97\n"
      "v_lshlrev_b32 v148, 16, v98\n"
      "v_and_b32 v149, s69, v98\n"
      "v_lshlrev_b32 v150, 16, v99\n"
      "v_and_b32 v151, s69, v99\n"
      "v_pk_mul_f32 v[144:145], v[144:145], v[208:209]\n"
      "v_pk_mul_f32 v[146:147], v[146:147], v[208:209]\n"
      "v_pk_mul_f32 v[148:149], v[148:149], v[208:209]\n"
      "v_pk_mul_f32 v[150:151], v[150:151], v[208:209]\n"
      "v_cvt_pk_f16_f32 v96, v144, v145\n"
      "v_cvt_pk_f16_f32 v97, v146, v147\n"
      "v_cvt_pk_f16_f32 v98, v148, v149\n"
      "v_cvt_pk_f16_f32 v99, v150, v151\n"
      "ds_write_b128 v206, v[96:99] offset:1088\n"
      "s_waitcnt vmcnt(10)\n"
      "v_lshlrev_b32 v152, 16, v100\n"
      "v_and_b32 v153, s69, v100\n"
      "v_lshlrev_b32 v154, 16, v101\n"
      "v_and_b32 v155, s69, v101\n"
      "v_lshlrev_b32 v156, 16, v102\n"
      "v_and_b32 v157, s69, v102\n"
      "v_lshlrev_b32 v158, 16, v103\n"
      "v_and_b32 v159, s69, v103\n"
      "v_pk_mul_f32 v[152:153], v[152:153], v[208:209]\n"
      "v_pk_mul_f32 v[154:155], v[154:155], v[208:209]\n"
      "v_pk_mul_f32 v[156:157], v[156:157], v[208:209]\n"
      "v_pk_mul_f32 v[158:159], v[158:159], v[208:209]\n"
      "v_cvt_pk_f16_f32 v100, v152, v153\n"
      "v_cvt_pk_f16_f32 v101, v154, v155\n"
      "v_cvt_pk_f16_f32 v102, v156, v157\n"
      "v_cvt_pk_f16_f32 v103, v158, v159\n"
      "ds_write_b128 v206, v[100:103] offset:1360\n"
      "s_waitcnt vmcnt(9)\n"
      "v_lshlrev_b32 v144, 16, v104\n"
      "v_and_b32 v145, s69, v104\n"
      "v_lshlrev_b32 v146, 16, v105\n"
      "v_and_b32 v147, s69, v105\n"
      "v_lshlrev_b32 v148, 16, v106\n"
      "v_and_b32 v149, s69, v106\n"
      "v_lshlrev_b32 v150, 16, v107\n"
      "v_and_b32 v151, s69, v107\n"
      "v_pk_mul_f32 v[144:145], v[144:145], v[208:209]\n"
      "v_pk_mul_f32 v[146:147], v[146:147], v[208:209]\n"
      "v_pk_mul_f32 v[148:149], v[148:149], v[208:209]\n"
      "v_pk_mul_f32 v[150:151], v[150:151], v[208:209]\n"
      "v_cvt_pk_f16_f32 v104, v144, v145\n"
      "v_cvt_pk_f16_f32 v105, v146, v147\n"
      "v_cvt_pk_f16_f32 v106, v148, v149\n"
      "v_cvt_pk_f16_f32 v107, v150, v151\n"
      "ds_write_b128 v206, v[104:107] offset:1632\n"
      "s_waitcnt vmcnt(8)\n"
      "v_lshlrev_b32 v152, 16, v108\n"
      "v_and_b32 v153, s69, v108\n"
      "v_lshlrev_b32 v154, 16, v109\n"
      "v_and_b32 v155, s69, v109\n"
      "v_lshlrev_b32 v156, 16, v110\n"
      "v_and_b32 v157, s69, v110\n"
      "v_lshlrev_b32 v158, 16, v111\n"
      "v_and_b32 v159, s69, v111\n"
      "v_pk_mul_f32 v[152:153], v[152:153], v[208:209]\n"
      "v_pk_mul_f32 v[154:155], v[154:155], v[208:209]\n"
      "v_pk_mul_f32 v[156:157], v[156:157], v[208:209]\n"
      "v_pk_mul_f32 v[158:159], v[158:159], v[208:209]\n"
      "v_cvt_pk_f16_f32 v108, v152, v153\n"
      "v_cvt_pk_f16_f32 v109, v154, v155\n"
      "v_cvt_pk_f16_f32 v110, v156, v157\n"
      "v_cvt_pk_f16_f32 v111, v158, v159\n"
      "ds_write_b128 v206, v[108:111] offset:1904\n"
      "s_waitcnt lgkmcnt(0)\n"
      "s_barrier\n"
      "v_lshlrev_b32 v242, 2, v248\n"
      "v_add_u32 v202, s64, v200\n"
      "v_add_u32 v242, 160512, v242\n"
      "v_add_u32 v201, s62, v200\n"
      "ds_read_b32 v242, v242\n"
      "ds_read_b128 v[112:115], v202 offset:0\n"
      "ds_read_b128 v[116:119], v202 offset:64\n"
      "ds_read_b128 v[120:123], v202 offset:128\n"
      "ds_read_b128 v[124:127], v202 offset:192\n"
      "ds_read_b128 v[176:179], v201 offset:0\n"
      "ds_read_b128 v[180:183], v201 offset:64\n"
      "ds_read_b128 v[184:187], v201 offset:128\n"
      "ds_read_b128 v[188:191], v201 offset:192\n"
      "ds_read_b128 v[128:131], v202 offset:272\n"
      "ds_read_b128 v[132:135], v202 offset:336\n"
      "ds_read_b128 v[136:139], v202 offset:400\n"
      "ds_read_b128 v[140:143], v202 offset:464\n"
      "ds_read_b128 v[144:147], v202 offset:544\n"
      "ds_read_b128 v[148:151], v202 offset:608\n"
      "s_waitcnt lgkmcnt(14)\n"
      "v_mul_f32 v242, v242, v242\n"
      "ds_read_b128 v[152:155], v202 offset:672\n"
      "ds_read_b128 v[156:159], v202 offset:736\n"
      "ds_read_b128 v[160:163], v202 offset:816\n"
      "ds_read_b128 v[164:167], v202 offset:880\n"
      "ds_read_b128 v[168:171], v202 offset:944\n"
      "ds_read_b128 v[172:175], v202 offset:1008\n"
      "v_mul_f32 v242, s68, v242\n"
      "v_mov_b32 v243, v242\n"
      "v_pk_mul_f32 v[0:1], v[0:1], v[242:243]\n"
      "v_pk_mul_f32 v[2:3], v[2:3], v[242:243]\n"
      "v_pk_mul_f32 v[4:5], v[4:5], v[242:243]\n"
      "v_pk_mul_f32 v[6:7], v[6:7], v[242:243]\n"
      "v_pk_mul_f32 v[8:9], v[8:9], v[242:243]\n"
      "v_pk_mul_f32 v[10:11], v[10:11], v[242:243]\n"
      "v_pk_mul_f32 v[12:13], v[12:13], v[242:243]\n"
      "v_pk_mul_f32 v[14:15], v[14:15], v[242:243]\n"
      "v_pk_mul_f32 v[16:17], v[16:17], v[242:243]\n"
      "v_pk_mul_f32 v[18:19], v[18:19], v[242:243]\n"
      "v_pk_mul_f32 v[20:21], v[20:21], v[242:243]\n"
      "v_pk_mul_f32 v[22:23], v[22:23], v[242:243]\n"
      "v_pk_mul_f32 v[24:25], v[24:25], v[242:243]\n"
      "v_pk_mul_f32 v[26:27], v[26:27], v[242:243]\n"
      "v_pk_mul_f32 v[28:29], v[28:29], v[242:243]\n"
      "v_pk_mul_f32 v[30:31], v[30:31], v[242:243]\n"
      "s_waitcnt lgkmcnt(12)\n"
      "v_pk_mul_f16 v80, v112, v176\n"
      "v_pk_mul_f16 v81, v113, v177\n"
      "v_pk_mul_f16 v82, v114, v178\n"
      "v_pk_mul_f16 v83, v115, v179\n"
      "v_pk_mul_f16 v84, v116, v180\n"
      "v_pk_mul_f16 v85, v117, v181\n"
      "v_pk_mul_f16 v86, v118, v182\n"
      "v_pk_mul_f16 v87, v119, v183\n"
      "v_pk_mul_f16 v88, v120, v184\n"
      "v_pk_mul_f16 v89, v121, v185\n"
      "v_pk_mul_f16 v90, v122, v186\n"
      "v_pk_mul_f16 v91, v123, v187\n"
      "v_pk_mul_f16 v92, v124, v188\n"
      "v_pk_mul_f16 v93, v125, v189\n"
      "v_pk_mul_f16 v94, v126, v190\n"
      "v_pk_mul_f16 v95, v127, v191\n"
      "v_mov_b32 v199, v203\n"
      "s_mov_b32 m0, 0\n"
      "s_waitcnt lgkmcnt(0)\n"
      "s_movrels_b32 s60, s70\n"
      "s_bitcmp1_b32 s60, 18\n"
      "s_cbranch_scc1 .Lhead_d_%=\n"
      ".Lhead_s_%=:\n"
      "v_mfma_f32_16x16x32_f16 v[64:67], a[0:3], v[80:83], v[0:3]\n"
      "v_pk_mul_f16 v96, v128, v176\n"
      "v_fma_f32 v195, |v72|, v56, v195\n"
      "v_mfma_f32_16x16x32_f16 v[64:67], a[4:7], v[84:87], v[64:67]\n"
      "s_bfe_u32 s63, s60, 0xa0005\n"
      "v_fma_f32 v195, |v73|, v57, v195\n"
      "v_mfma_f32_16x16x32_f16 v[64:67], a[8:11], v[88:91], v[64:67]\n"
      "v_pk_mul_f16 v97, v129, v177\n"
      "v_fma_f32 v195, |v74|, v58, v195\n"
      "v_mfma_f32_16x16x32_f16 v[64:67], a[12:15], v[92:95], v[64:67]\n"
      "s_lshl_b32 s63, s63, 2\n"
      "v_fma_f32 v195, |v75|, v59, v195\n"
      "v_mfma_f32_16x16x32_f16 v[68:71], a[28:31], v[92:95], v[4:7]\n"
      "v_pk_mul_f16 v98, v130, v178\n"
      "v_fma_f32 v195, |v76|, v60, v195\n"
      "v_mfma_f32_16x16x32_f16 v[68:71], a[24:27], v[88:91], v[68:71]\n"
      "v_fma_f32 v195, |v77|, v61, v195\n"
      "v_mfma_f32_16x16x32_f16 v[68:71], a[20:23], v[84:87], v[68:71]\n"
      "v_pk_mul_f16 v99, v131, v179\n"
      "v_fma_f32 v195, |v78|, v62, v195\n"
      "v_mfma_f32_16x16x32_f16 v[68:71], a[16:19], v[80:83], v[68:71]\n"
      "v_fma_f32 v195, |v79|, v63, v195\n"
      "v_mfma_f32_16x16x32_f16 v[72:75], a[32:35], v[80:83], v[8:11]\n"
      "v_pk_mul_f16 v100, v132, v180\n"
      "v_mul_f32_e64 v192, |v64|, v32\n"
      "v_mfma_f32_16x16x32_f16 v[72:75], a[36:39], v[84:87], v[72:75]\n"
      "v_permlane32_swap_b32 v193, v195\n"
      "v_fma_f32 v192, |v65|, v33, v192\n"
      "v_mfma_f32_16x16x32_f16 v[72:75], a[40:43], v[88:91], v[72:75]\n"
      "v_pk_mul_f16 v101, v133, v181\n"
      "v_fma_f32 v192, |v66|, v34, v192\n"
      "v_mfma_f32_16x16x32_f16 v[72:75], a[44:47], v[92:95], v[72:75]\n"
      "v_add_f32 v197, v193, v195\n"
      "v_fma_f32 v192, |v67|, v35, v192\n"
      "v_mfma_f32_16x16x32_f16 v[76:79], a[60:63], v[92:95], v[12:15]\n"
      "v_pk_mul_f16 v102, v134, v182\n"
      "v_fma_f32 v192, |v68|, v36, v192\n"
      "v_mfma_f32_16x16x32_f16 v[76:79], a[56:59], v[88:91], v[76:79]\n"
      "v_fma_f32 v192, |v69|, v37, v192\n"
      "v_mfma_f32_16x16x32_f16 v[76:79], a[52:55], v[84:87], v[76:79]\n"
      "v_pk_mul_f16 v103, v135, v183\n"
      "v_fma_f32 v192, |v70|, v38, v192\n"
      "v_mfma_f32_16x16x32_f16 v[76:79], a[48:51], v[80:83], v[76:79]\n"
      "v_permlane16_swap_b32 v196, v197\n"
      "v_fma_f32 v192, |v71|, v39, v192\n"
      "v_mfma_f32_16x16x32_f16 v[64:67], a[64:67], v[80:83], v[16:19]\n"
      "v_pk_mul_f16 v104, v136, v184\n"
      "v_fma_f32 v192, |v72|, v40, v192\n"
      "v_mfma_f32_16x16x32_f16 v[64:67], a[68:71], v[84:87], v[64:67]\n"
      "v_add_f32 v198, v196, v197\n"
      "v_fma_f32 v192, |v73|, v41, v192\n"
      "v_mfma_f32_16x16x32_f16 v[64:67], a[72:75], v[88:91], v[64:67]\n"
      "v_pk_mul_f16 v105, v137, v185\n"
      "v_fma_f32 v192, |v74|, v42, v192\n"
      "v_mfma_f32_16x16x32_f16 v[64:67], a[76:79], v[92:95], v[64:67]\n"
      "ds_write_b32 v199, v198\n"
      "v_fma_f32 v192, |v75|, v43, v192\n"
      "v_mfma_f32_16x16x32_f16 v[68:71], a[92:95], v[92:95], v[20:23]\n"
      "v_pk_mul_f16 v106, v138, v186\n"
      "v_fma_f32 v192, |v76|, v44, v192\n"
      "v_mfma_f32_16x16x32_f16 v[68:71], a[88:91], v[88:91], v[68:71]\n"
      "v_fma_f32 v192, |v77|, v45, v192\n"
      "v_mfma_f32_16x16x32_f16 v[68:71], a[84:87], v[84:87], v[68:71]\n"
      "v_pk_mul_f16 v107, v139, v187\n"
      "v_fma_f32 v192, |v78|, v46, v192\n"
      "v_mfma_f32_16x16x32_f16 v[68:71], a[80:83], v[80:83], v[68:71]\n"
      "v_fma_f32 v192, |v79|, v47, v192\n"
      "v_mfma_f32_16x16x32_f16 v[72:75], a[96:99], v[80:83], v[24:27]\n"
      "v_pk_mul_f16 v108, v140, v188\n"
      "v_fma_f32 v192, |v64|, v48, v192\n"
      "v_mfma_f32_16x16x32_f16 v[72:75], a[100:103], v[84:87], v[72:75]\n"
      "v_fma_f32 v192, |v65|, v49, v192\n"
      "v_mfma_f32_16x16x32_f16 v[72:75], a[104:107], v[88:91], v[72:75]\n"
      "v_pk_mul_f16 v109, v141, v189\n"
      "v_fma_f32 v192, |v66|, v50, v192\n"
      "v_mfma_f32_16x16x32_f16 v[72:75], a[108:111], v[92:95], v[72:75]\n"
      "v_fma_f32 v192, |v67|, v51, v192\n"
      "v_mfma_f32_16x16x32_f16 v[76:79], a[124:127], v[92:95], v[28:31]\n"
      "v_pk_mul_f16 v110, v142, v190\n"
      "v_fma_f32 v192, |v68|, v52, v192\n"
      "v_mfma_f32_16x16x32_f16 v[76:79], a[120:123], v[88:91], v[76:79]\n"
      "v_fma_f32 v192, |v69|, v53, v192\n"
      "v_mfma_f32_16x16x32_f16 v[76:79], a[116:119], v[84:87], v[76:79]\n"
      "v_pk_mul_f16 v111, v143, v191\n"
      "v_fma_f32 v192, |v70|, v54, v192\n"
      "v_mfma_f32_16x16x32_f16 v[76:79], a[112:115], v[80:83], v[76:79]\n"
      "v_fma_f32 v192, |v71|, v55, v192\n"
      "v_mfma_f32_16x16x32_f16 v[64:67], a[0:3], v[96:99], v[0:3]\n"
      "v_pk_mul_f16 v80, v144, v176\n"
      "v_fma_f32 v192, |v72|, v56, v192\n"
      "v_mfma_f32_16x16x32_f16 v[64:67], a[4:7], v[100:103], v[64:67]\n"
      "v_add_u32 v199, s63, v203\n"
      "v_fma_f32 v192, |v73|, v57, v192\n"
      "v_mfma_f32_16x16x32_f16 v[64:67], a[8:11], v[104:107], v[64:67]\n"
      "v_pk_mul_f16 v81, v145, v177\n"
      "v_fma_f32 v192, |v74|, v58, v192\n"
      "v_mfma_f32_16x16x32_f16 v[64:67], a[12:15], v[108:111], v[64:67]\n"
      "v_fma_f32 v192, |v75|, v59, v192\n"
      "v_mfma_f32_16x16x32_f16 v[68:71], a[28:31], v[108:111], v[4:7]\n"
      "v_pk_mul_f16 v82, v146, v178\n"
      "v_fma_f32 v192, |v76|, v60, v192\n"
      "v_mfma_f32_16x16x32_f16 v[68:71], a[24:27], v[104:107], v[68:71]\n"
      "s_and_b32 s62, s60, 31\n"
      "v_fma_f32 v192, |v77|, v61, v192\n"
      "v_mfma_f32_16x16x32_f16 v[68:71], a[20:23], v[100:103], v[68:71]\n"
      "v_pk_mul_f16 v83, v147, v179\n"
      "v_fma_f32 v192, |v78|, v62, v192\n"
      "v_mfma_f32_16x16x32_f16 v[68:71], a[16:19], v[96:99], v[68:71]\n"
      "s_mul_i32 s62, s62, 272\n"
      "v_fma_f32 v192, |v79|, v63, v192\n"
      "v_mfma_f32_16x16x32_f16 v[72:75], a[32:35], v[96:99], v[8:11]\n"
      "v_pk_mul_f16 v84, v148, v180\n"
      "v_mul_f32_e64 v193, |v64|, v32\n"
      "v_mfma_f32_16x16x32_f16 v[72:75], a[36:39], v[100:103], v[72:75]\n"
      "v_fma_f32 v193, |v65|, v33, v193\n"
      "v_mfma_f32_16x16x32_f16 v[72:75], a[40:43], v[104:107], v[72:75]\n"
      "v_pk_mul_f16 v85, v149, v181\n"
      "v_fma_f32 v193, |v66|, v34, v193\n"
      "v_mfma_f32_16x16x32_f16 v[72:75], a[44:47], v[108:111], v[72:75]\n"
      "v_fma_f32 v193, |v67|, v35, v193\n"
      "v_mfma_f32_16x16x32_f16 v[76:79], a[60:63], v[108:111], v[12:15]\n"
      "v_pk_mul_f16 v86, v150, v182\n"
      "v_fma_f32 v193, |v68|, v36, v193\n"
      "v_mfma_f32_16x16x32_f16 v[76:79], a[56:59], v[104:107], v[76:79]\n"
      "v_fma_f32 v193, |v69|, v37, v193\n"
      "v_mfma_f32_16x16x32_f16 v[76:79], a[52:55], v[100:103], v[76:79]\n"
      "v_pk_mul_f16 v87, v151, v183\n"
      "v_fma_f32 v193, |v70|, v38, v193\n"
      "v_mfma_f32_16x16x32_f16 v[76:79], a[48:51], v[96:99], v[76:79]\n"
      "v_fma_f32 v193, |v71|, v39, v193\n"
      "v_mfma_f32_16x16x32_f16 v[64:67], a[64:67], v[96:99], v[16:19]\n"
      "v_pk_mul_f16 v88, v152, v184\n"
      "v_fma_f32 v193, |v72|, v40, v193\n"
      "v_mfma_f32_16x16x32_f16 v[64:67], a[68:71], v[100:103], v[64:67]\n"
      "v_fma_f32 v193, |v73|, v41, v193\n"
      "v_mfma_f32_16x16x32_f16 v[64:67], a[72:75], v[104:107], v[64:67]\n"
      "v_pk_mul_f16 v89, v153, v185\n"
      "v_fma_f32 v193, |v74|, v42, v193\n"
      "v_mfma_f32_16x16x32_f16 v[64:67], a[76:79], v[108:111], v[64:67]\n"
      "v_fma_f32 v193, |v75|, v43, v193\n"
      "v_mfma_f32_16x16x32_f16 v[68:71], a[92:95], v[108:111], v[20:23]\n"
      "v_pk_mul_f16 v90, v154, v186\n"
      "v_fma_f32 v193, |v76|, v44, v193\n"
      "v_mfma_f32_16x16x32_f16 v[68:71], a[88:91], v[104:107], v[68:71]\n"
      "v_fma_f32 v193, |v77|, v45, v193\n"
      "v_mfma_f32_16x16x32_f16 v[68:71], a[84:87], v[100:103], v[68:71]\n"
      "v_pk_mul_f16 v91, v155, v187\n"
      "v_fma_f32 v193, |v78|, v46, v193\n"
      "v_mfma_f32_16x16x32_f16 v[68:71], a[80:83], v[96:99], v[68:71]\n"
      "v_fma_f32 v193, |v79|, v47, v193\n"
      "v_mfma_f32_16x16x32_f16 v[72:75], a[96:99], v[96:99], v[24:27]\n"
      "v_pk_mul_f16 v92, v156, v188\n"
      "v_fma_f32 v193, |v64|, v48, v193\n"
      "v_mfma_f32_16x16x32_f16 v[72:75], a[100:103], v[100:103], v[72:75]\n"
      "v_fma_f32 v193, |v65|, v49, v193\n"
      "v_mfma_f32_16x16x32_f16 v[72:75], a[104:107], v[104:107], v[72:75]\n"
      "v_pk_mul_f16 v93, v157, v189\n"
      "v_fma_f32 v193, |v66|, v50, v193\n"
      "v_mfma_f32_16x16x32_f16 v[72:75], a[108:111], v[108:111], v[72:75]\n"
      "v_fma_f32 v193, |v67|, v51, v193\n"
      "v_mfma_f32_16x16x32_f16 v[76:79], a[124:127], v[108:111], v[28:31]\n"
      "v_pk_mul_f16 v94, v158, v190\n"
      "v_fma_f32 v193, |v68|, v52, v193\n"
      "v_mfma_f32_16x16x32_f16 v[76:79], a[120:123], v[104:107], v[76:79]\n"
      "v_fma_f32 v193, |v69|, v53, v193\n"
      "v_mfma_f32_16x16x32_f16 v[76:79], a[116:119], v[100:103], v[76:79]\n"
      "v_pk_mul_f16 v95, v159, v191\n"
      "v_fma_f32 v193, |v70|, v54, v193\n"
      "v_mfma_f32_16x16x32_f16 v[76:79], a[112:115], v[96:99], v[76:79]\n"
      "v_fma_f32 v193, |v71|, v55, v193\n"
      "v_mfma_f32_16x16x32_f16 v[64:67], a[0:3], v[80:83], v[0:3]\n"
      "v_pk_mul_f16 v96, v160, v176\n"
      "v_fma_f32 v193, |v72|, v56, v193\n"
      "v_mfma_f32_16x16x32_f16 v[64:67], a[4:7], v[84:87], v[64:67]\n"
      "v_fma_f32 v193, |v73|, v57, v193\n"
      "v_mfma_f32_16x16x32_f16 v[64:67], a[8:11], v[88:91], v[64:67]\n"
      "v_pk_mul_f16 v97, v161, v177\n"
      "v_fma_f32 v193, |v74|, v58, v193\n"
      "v_mfma_f32_16x16x32_f16 v[64:67], a[12:15], v[92:95], v[64:67]\n"
      "v_fma_f32 v193, |v75|, v59, v193\n"
      "v_mfma_f32_16x16x32_f16 v[68:71], a[28:31], v[92:95], v[4:7]\n"
      "v_pk_mul_f16 v98, v162, v178\n"
      "v_fma_f32 v193, |v76|, v60, v193\n"
      "v_mfma_f32_16x16x32_f16 v[68:71], a[24:27], v[88:91], v[68:71]\n"
      "v_fma_f32 v193, |v77|, v61, v193\n"
      "v_mfma_f32_16x16x32_f16 v[68:71], a[20:23], v[84:87], v[68:71]\n"
      "v_pk_mul_f16 v99, v163, v179\n"
      "v_fma_f32 v193, |v78|, v62, v193\n"
      "v_mfma_f32_16x16x32_f16 v[68:71], a[16:19], v[80:83], v[68:71]\n"
      "v_fma_f32 v193, |v79|, v63, v193\n"
      "v_mfma_f32_16x16x32_f16 v[72:75], a[32:35], v[80:83], v[8:11]\n"
      "v_pk_mul_f16 v100, v164, v180\n"
      "v_mul_f32_e64 v194, |v64|, v32\n"
      "v_mfma_f32_16x16x32_f16 v[72:75], a[36:39], v[84:87], v[72:75]\n"
      "v_fma_f32 v194, |v65|, v33, v194\n"
      "v_mfma_f32_16x16x32_f16 v[72:75], a[40:43], v[88:91], v[72:75]\n"
      "v_pk_mul_f16 v101, v165, v181\n"
      "v_fma_f32 v194, |v66|, v34, v194\n"
      "v_mfma_f32_16x16x32_f16 v[72:75], a[44:47], v[92:95], v[72:75]\n"
      "v_fma_f32 v194, |v67|, v35, v194\n"
      "v_mfma_f32_16x16x32_f16 v[76:79], a[60:63], v[92:95], v[12:15]\n"
      "v_pk_mul_f16 v102, v166, v182\n"
      "v_fma_f32 v194, |v68|, v36, v194\n"
      "v_mfma_f32_16x16x32_f16 v[76:79], a[56:59], v[88:91], v[76:79]\n"
      "v_fma_f32 v194, |v69|, v37, v194\n"
      "v_mfma_f32_16x16x32_f16 v[76:79], a[52:55], v[84:87], v[76:79]\n"
      "v_pk_mul_f16 v103, v167, v183\n"
      "v_fma_f32 v194, |v70|, v38, v194\n"
      "v_mfma_f32_16x16x32_f16 v[76:79], a[48:51], v[80:83], v[76:79]\n"
      "v_fma_f32 v194, |v71|, v39, v194\n"
      "v_mfma_f32_16x16x32_f16 v[64:67], a[64:67], v[80:83], v[16:19]\n"
      "v_pk_mul_f16 v104, v168, v184\n"
      "v_fma_f32 v194, |v72|, v40, v194\n"
      "v_mfma_f32_16x16x32_f16 v[64:67], a[68:71], v[84:87], v[64:67]\n"
      "v_fma_f32 v194, |v73|, v41, v194\n"
      "v_mfma_f32_16x16x32_f16 v[64:67], a[72:75], v[88:91], v[64:67]\n"
      "v_pk_mul_f16 v105, v169, v185\n"
      "v_fma_f32 v194, |v74|, v42, v194\n"
      "v_mfma_f32_16x16x32_f16 v[64:67], a[76:79], v[92:95], v[64:67]\n"
      "v_fma_f32 v194, |v75|, v43, v194\n"
      "v_mfma_f32_16x16x32_f16 v[68:71], a[92:95], v[92:95], v[20:23]\n"
      "v_pk_mul_f16 v106, v170, v186\n"
      "v_fma_f32 v194, |v76|, v44, v194\n"
      "v_mfma_f32_16x16x32_f16 v[68:71], a[88:91], v[88:91], v[68:71]\n"
      "v_fma_f32 v194, |v77|, v45, v194\n"
      "v_mfma_f32_16x16x32_f16 v[68:71], a[84:87], v[84:87], v[68:71]\n"
      "v_pk_mul_f16 v107, v171, v187\n"
      "v_fma_f32 v194, |v78|, v46, v194\n"
      "v_mfma_f32_16x16x32_f16 v[68:71], a[80:83], v[80:83], v[68:71]\n"
      "v_fma_f32 v194, |v79|, v47, v194\n"
      "v_mfma_f32_16x16x32_f16 v[72:75], a[96:99], v[80:83], v[24:27]\n"
      "v_pk_mul_f16 v108, v172, v188\n"
      "v_fma_f32 v194, |v64|, v48, v194\n"
      "v_mfma_f32_16x16x32_f16 v[72:75], a[100:103], v[84:87], v[72:75]\n"
      "v_fma_f32 v194, |v65|, v49, v194\n"
      "v_mfma_f32_16x16x32_f16 v[72:75], a[104:107], v[88:91], v[72:75]\n"
      "v_pk_mul_f16 v109, v173, v189\n"
      "v_fma_f32 v194, |v66|, v50, v194\n"
      "v_mfma_f32_16x16x32_f16 v[72:75], a[108:111], v[92:95], v[72:75]\n"
      "v_fma_f32 v194, |v67|, v51, v194\n"
      "v_mfma_f32_16x16x32_f16 v[76:79], a[124:127], v[92:95], v[28:31]\n"
      "v_pk_mul_f16 v110, v174, v190\n"
      "v_fma_f32 v194, |v68|, v52, v194\n"
      "v_mfma_f32_16x16x32_f16 v[76:79], a[120:123], v[88:91], v[76:79]\n"
      "s_and_b32 s52, s60, 0x1f00000\n"
      "v_fma_f32 v194, |v69|, v53, v194\n"
      "v_mfma_f32_16x16x32_f16 v[76:79], a[116:119], v[84:87], v[76:79]\n"
      "v_pk_mul_f16 v111, v175, v191\n"
      "v_fma_f32 v194, |v70|, v54, v194\n"
      "v_mfma_f32_16x16x32_f16 v[76:79], a[112:115], v[80:83], v[76:79]\n"
      "v_fma_f32 v194, |v71|, v55, v194\n"
      ".Lmid_%=:\n"
      "s_cbranch_scc1 .Lmidspecial_%=\n"
      ".Llast_%=:\n"
      "v_mfma_f32_16x16x32_f16 v[64:67], a[0:3], v[96:99], v[0:3]\n"
      "v_add_u32 v201, s62, v200\n"
      "v_fma_f32 v194, |v72|, v56, v194\n"
      "v_mfma_f32_16x16x32_f16 v[64:67], a[4:7], v[100:103], v[64:67]\n"
      "ds_read_b128 v[176:179], v201 offset:0\n"
      "v_fma_f32 v194, |v73|, v57, v194\n"
      "v_mfma_f32_16x16x32_f16 v[64:67], a[8:11], v[104:107], v[64:67]\n"
      "ds_read_b128 v[180:183], v201 offset:64\n"
      "v_fma_f32 v194, |v74|, v58, v194\n"
      "v_mfma_f32_16x16x32_f16 v[64:67], a[12:15], v[108:111], v[64:67]\n"
      "ds_read_b128 v[184:187], v201 offset:128\n"
      "v_fma_f32 v194, |v75|, v59, v194\n"
      "v_mfma_f32_16x16x32_f16 v[68:71], a[28:31], v[108:111], v[4:7]\n"
      "ds_read_b128 v[188:191], v201 offset:192\n"
      "v_fma_f32 v194, |v76|, v60, v194\n"
      "v_mfma_f32_16x16x32_f16 v[68:71], a[24:27], v[104:107], v[68:71]\n"
      "s_add_u32 m0, m0, 1\n"
      "v_fma_f32 v194, |v77|, v61, v194\n"
      "v_mfma_f32_16x16x32_f16 v[68:71], a[20:23], v[100:103], v[68:71]\n"
      "s_and_b32 s53, s60, 0x2000000\n"
      "v_fma_f32 v194, |v78|, v62, v194\n"
      "v_mfma_f32_16x16x32_f16 v[68:71], a[16:19], v[96:99], v[68:71]\n"
      "s_movrels_b32 s61, s70\n"
      "v_fma_f32 v194, |v79|, v63, v194\n"
      "v_mfma_f32_16x16x32_f16 v[72:75], a[32:35], v[96:99], v[8:11]\n"
      "s_and_b32 s54, s61, 0xc0000\n"
      "v_mul_f32_e64 v195, |v64|, v32\n"
      "v_mfma_f32_16x16x32_f16 v[72:75], a[36:39], v[100:103], v[72:75]\n"
      "v_permlane32_swap_b32 v192, v194\n"
      "v_fma_f32 v195, |v65|, v33, v195\n"
      "v_mfma_f32_16x16x32_f16 v[72:75], a[40:43], v[104:107], v[72:75]\n"
      "s_mov_b32 s60, s61\n"
      "v_fma_f32 v195, |v66|, v34, v195\n"
      "v_mfma_f32_16x16x32_f16 v[72:75], a[44:47], v[108:111], v[72:75]\n"
      "v_add_f32 v196, v192, v194\n"
      "v_fma_f32 v195, |v67|, v35, v195\n"
      "v_mfma_f32_16x16x32_f16 v[76:79], a[60:63], v[108:111], v[12:15]\n"
      "s_or_b32 s53, s53, s54\n"
      "v_fma_f32 v195, |v68|, v36, v195\n"
      "v_mfma_f32_16x16x32_f16 v[76:79], a[56:59], v[104:107], v[76:79]\n"
      "v_fma_f32 v195, |v69|, v37, v195\n"
      "v_mfma_f32_16x16x32_f16 v[76:79], a[52:55], v[100:103], v[76:79]\n"
      "s_waitcnt lgkmcnt(0)\n"
      "v_fma_f32 v195, |v70|, v38, v195\n"
      "v_mfma_f32_16x16x32_f16 v[76:79], a[48:51], v[96:99], v[76:79]\n"
      "v_fma_f32 v195, |v71|, v39, v195\n"
      "v_mfma_f32_16x16x32_f16 v[64:67], a[64:67], v[96:99], v[16:19]\n"
      "v_pk_mul_f16 v80, v112, v176\n"
      "v_fma_f32 v195, |v72|, v40, v195\n"
      "v_mfma_f32_16x16x32_f16 v[64:67], a[68:71], v[100:103], v[64:67]\n"
      "v_pk_mul_f16 v81, v113, v177\n"
      "v_fma_f32 v195, |v73|, v41, v195\n"
      "v_mfma_f32_16x16x32_f16 v[64:67], a[72:75], v[104:107], v[64:67]\n"
      "v_pk_mul_f16 v82, v114, v178\n"
      "v_fma_f32 v195, |v74|, v42, v195\n"
      "v_mfma_f32_16x16x32_f16 v[64:67], a[76:79], v[108:111], v[64:67]\n"
      "v_pk_mul_f16 v83, v115, v179\n"
      "v_fma_f32 v195, |v75|, v43, v195\n"
      "v_mfma_f32_16x16x32_f16 v[68:71], a[92:95], v[108:111], v[20:23]\n"
      "v_pk_mul_f16 v84, v116, v180\n"
      "v_fma_f32 v195, |v76|, v44, v195\n"
      "v_mfma_f32_16x16x32_f16 v[68:71], a[88:91], v[104:107], v[68:71]\n"
      "v_pk_mul_f16 v85, v117, v181\n"
      "v_fma_f32 v195, |v77|, v45, v195\n"
      "v_mfma_f32_16x16x32_f16 v[68:71], a[84:87], v[100:103], v[68:71]\n"
      "v_pk_mul_f16 v86, v118, v182\n"
      "v_fma_f32 v195, |v78|, v46, v195\n"
      "v_mfma_f32_16x16x32_f16 v[68:71], a[80:83], v[96:99], v[68:71]\n"
      "v_pk_mul_f16 v87, v119, v183\n"
      "v_fma_f32 v195, |v79|, v47, v195\n"
      "v_mfma_f32_16x16x32_f16 v[72:75], a[96:99], v[96:99], v[24:27]\n"
      "v_pk_mul_f16 v88, v120, v184\n"
      "v_fma_f32 v195, |v64|, v48, v195\n"
      "v_mfma_f32_16x16x32_f16 v[72:75], a[100:103], v[100:103], v[72:75]\n"
      "v_pk_mul_f16 v89, v121, v185\n"
      "v_fma_f32 v195, |v65|, v49, v195\n"
      "v_mfma_f32_16x16x32_f16 v[72:75], a[104:107], v[104:107], v[72:75]\n"
      "v_pk_mul_f16 v90, v122, v186\n"
      "v_fma_f32 v195, |v66|, v50, v195\n"
      "v_mfma_f32_16x16x32_f16 v[72:75], a[108:111], v[108:111], v[72:75]\n"
      "v_pk_mul_f16 v91, v123, v187\n"
      "v_fma_f32 v195, |v67|, v51, v195\n"
      "v_mfma_f32_16x16x32_f16 v[76:79], a[124:127], v[108:111], v[28:31]\n"
      "v_pk_mul_f16 v92, v124, v188\n"
      "v_fma_f32 v195, |v68|, v52, v195\n"
      "v_mfma_f32_16x16x32_f16 v[76:79], a[120:123], v[104:107], v[76:79]\n"
      "v_pk_mul_f16 v93, v125, v189\n"
      "v_fma_f32 v195, |v69|, v53, v195\n"
      "v_mfma_f32_16x16x32_f16 v[76:79], a[116:119], v[100:103], v[76:79]\n"
      "v_pk_mul_f16 v94, v126, v190\n"
      "v_fma_f32 v195, |v70|, v54, v195\n"
      "v_mfma_f32_16x16x32_f16 v[76:79], a[112:115], v[96:99], v[76:79]\n"
      "v_pk_mul_f16 v95, v127, v191\n"
      "v_fma_f32 v195, |v71|, v55, v195\n"
      "s_cbranch_scc0 .Lhead_s_%=\n"
      ".Ldispatch_%=:\n"
      "s_bitcmp1_b32 s53, 25\n"
      "s_cbranch_scc1 .Ldrain_%=\n"
      "s_bitcmp1_b32 s53, 19\n"
      "s_cbranch_scc1 .Ltail_%=\n"
      ".Lhead_d_%=:\n"
      "v_mfma_f32_16x16x32_f16 v[64:67], a[0:3], v[80:83], v[0:3]\n"
      "v_pk_mul_f16 v96, v144, v160\n"
      "v_fma_f32 v195, |v72|, v56, v195\n"
      "v_mfma_f32_16x16x32_f16 v[64:67], a[4:7], v[84:87], v[64:67]\n"
      "s_bfe_u32 s63, s60, 0xa0005\n"
      "v_fma_f32 v195, |v73|, v57, v195\n"
      "v_mfma_f32_16x16x32_f16 v[64:67], a[8:11], v[88:91], v[64:67]\n"
      "v_pk_mul_f16 v97, v145, v161\n"
      "v_fma_f32 v195, |v74|, v58, v195\n"
      "v_mfma_f32_16x16x32_f16 v[64:67], a[12:15], v[92:95], v[64:67]\n"
      "s_lshl_b32 s63, s63, 2\n"
      "v_fma_f32 v195, |v75|, v59, v195\n"
      "v_mfma_f32_16x16x32_f16 v[68:71], a[28:31], v[92:95], v[4:7]\n"
      "v_pk_mul_f16 v98, v146, v162\n"
      "v_fma_f32 v195, |v76|, v60, v195\n"
      "v_mfma_f32_16x16x32_f16 v[68:71], a[24:27], v[88:91], v[68:71]\n"
      "v_fma_f32 v195, |v77|, v61, v195\n"
      "v_mfma_f32_16x16x32_f16 v[68:71], a[20:23], v[84:87], v[68:71]\n"
      "v_pk_mul_f16 v99, v147, v163\n"
      "v_fma_f32 v195, |v78|, v62, v195\n"
      "v_mfma_f32_16x16x32_f16 v[68:71], a[16:19], v[80:83], v[68:71]\n"
      "v_fma_f32 v195, |v79|, v63, v195\n"
      "v_mfma_f32_16x16x32_f16 v[72:75], a[32:35], v[80:83], v[8:11]\n"
      "v_pk_mul_f16 v100, v148, v164\n"
      "v_mul_f32_e64 v194, |v64|, v32\n"
      "v_mfma_f32_16x16x32_f16 v[72:75], a[36:39], v[84:87], v[72:75]\n"
      "v_permlane32_swap_b32 v193, v195\n"
      "v_fma_f32 v194, |v65|, v33, v194\n"
      "v_mfma_f32_16x16x32_f16 v[72:75], a[40:43], v[88:91], v[72:75]\n"
      "v_pk_mul_f16 v101, v149, v165\n"
      "v_fma_f32 v194, |v66|, v34, v194\n"
      "v_mfma_f32_16x16x32_f16 v[72:75], a[44:47], v[92:95], v[72:75]\n"
      "v_add_f32 v197, v193, v195\n"
      "v_fma_f32 v194, |v67|, v35, v194\n"
      "v_mfma_f32_16x16x32_f16 v[76:79], a[60:63], v[92:95], v[12:15]\n"
      "v_pk_mul_f16 v102, v150, v166\n"
      "v_fma_f32 v194, |v68|, v36, v194\n"
      "v_mfma_f32_16x16x32_f16 v[76:79], a[56:59], v[88:91], v[76:79]\n"
      "v_fma_f32 v194, |v69|, v37, v194\n"
      "v_mfma_f32_16x16x32_f16 v[76:79], a[52:55], v[84:87], v[76:79]\n"
      "v_pk_mul_f16 v103, v151, v167\n"
      "v_fma_f32 v194, |v70|, v38, v194\n"
      "v_mfma_f32_16x16x32_f16 v[76:79], a[48:51], v[80:83], v[76:79]\n"
      "v_permlane16_swap_b32 v196, v197\n"
      "v_fma_f32 v194, |v71|, v39, v194\n"
      "v_mfma_f32_16x16x32_f16 v[64:67], a[64:67], v[80:83], v[16:19]\n"
      "v_pk_mul_f16 v104, v152, v168\n"
      "v_fma_f32 v194, |v72|, v40, v194\n"
      "v_mfma_f32_16x16x32_f16 v[64:67], a[68:71], v[84:87], v[64:67]\n"
      "v_add_f32 v198, v196, v197\n"
      "v_fma_f32 v194, |v73|, v41, v194\n"
      "v_mfma_f32_16x16x32_f16 v[64:67], a[72:75], v[88:91], v[64:67]\n"
      "v_pk_mul_f16 v105, v153, v169\n"
      "v_fma_f32 v194, |v74|, v42, v194\n"
      "v_mfma_f32_16x16x32_f16 v[64:67], a[76:79], v[92:95], v[64:67]\n"
      "ds_write_b32 v199, v198\n"
      "v_fma_f32 v194, |v75|, v43, v194\n"
      "v_mfma_f32_16x16x32_f16 v[68:71], a[92:95], v[92:95], v[20:23]\n"
      "v_pk_mul_f16 v106, v154, v170\n"
      "v_fma_f32 v194, |v76|, v44, v194\n"
      "v_mfma_f32_16x16x32_f16 v[68:71], a[88:91], v[88:91], v[68:71]\n"
      "v_fma_f32 v194, |v77|, v45, v194\n"
      "v_mfma_f32_16x16x32_f16 v[68:71], a[84:87], v[84:87], v[68:71]\n"
      "v_pk_mul_f16 v107, v155, v171\n"
      "v_fma_f32 v194, |v78|, v46, v194\n"
      "v_mfma_f32_16x16x32_f16 v[68:71], a[80:83], v[80:83], v[68:71]\n"
      "v_fma_f32 v194, |v79|, v47, v194\n"
      "v_mfma_f32_16x16x32_f16 v[72:75], a[96:99], v[80:83], v[24:27]\n"
      "v_pk_mul_f16 v108, v156, v172\n"
      "v_fma_f32 v194, |v64|, v48, v194\n"
      "v_mfma_f32_16x16x32_f16 v[72:75], a[100:103], v[84:87], v[72:75]\n"
      "v_fma_f32 v194, |v65|, v49, v194\n"
      "v_mfma_f32_16x16x32_f16 v[72:75], a[104:107], v[88:91], v[72:75]\n"
      "v_pk_mul_f16 v109, v157, v173\n"
      "v_fma_f32 v194, |v66|, v50, v194\n"
      "v_mfma_f32_16x16x32_f16 v[72:75], a[108:111], v[92:95], v[72:75]\n"
      "v_fma_f32 v194, |v67|, v51, v194\n"
      "v_mfma_f32_16x16x32_f16 v[76:79], a[124:127], v[92:95], v[28:31]\n"
      "v_pk_mul_f16 v110, v158, v174\n"
      "v_fma_f32 v194, |v68|, v52, v194\n"
      "v_mfma_f32_16x16x32_f16 v[76:79], a[120:123], v[88:91], v[76:79]\n"
      "v_fma_f32 v194, |v69|, v53, v194\n"
      "v_mfma_f32_16x16x32_f16 v[76:79], a[116:119], v[84:87], v[76:79]\n"
      "v_pk_mul_f16 v111, v159, v175\n"
      "v_fma_f32 v194, |v70|, v54, v194\n"
      "v_mfma_f32_16x16x32_f16 v[76:79], a[112:115], v[80:83], v[76:79]\n"
      "v_fma_f32 v194, |v71|, v55, v194\n"
      "v_mfma_f32_16x16x32_f16 v[64:67], a[0:3], v[96:99], v[0:3]\n"
      "v_pk_mul_f16 v80, v112, v128\n"
      "v_fma_f32 v194, |v72|, v56, v194\n"
      "v_mfma_f32_16x16x32_f16 v[64:67], a[4:7], v[100:103], v[64:67]\n"
      "v_add_u32 v199, s63, v204\n"
      "v_fma_f32 v194, |v73|, v57, v194\n"
      "v_mfma_f32_16x16x32_f16 v[64:67], a[8:11], v[104:107], v[64:67]\n"
      "v_pk_mul_f16 v81, v113, v129\n"
      "v_fma_f32 v194, |v74|, v58, v194\n"
      "v_mfma_f32_16x16x32_f16 v[64:67], a[12:15], v[108:111], v[64:67]\n"
      "v_fma_f32 v194, |v75|, v59, v194\n"
      "v_mfma_f32_16x16x32_f16 v[68:71], a[28:31], v[108:111], v[4:7]\n"
      "v_pk_mul_f16 v82, v114, v130\n"
      "v_fma_f32 v194, |v76|, v60, v194\n"
      "v_mfma_f32_16x16x32_f16 v[68:71], a[24:27], v[104:107], v[68:71]\n"
      "s_and_b32 s62, s60, 31\n"
      "v_fma_f32 v194, |v77|, v61, v194\n"
      "v_mfma_f32_16x16x32_f16 v[68:71], a[20:23], v[100:103], v[68:71]\n"
      "v_pk_mul_f16 v83, v115, v131\n"
      "v_fma_f32 v194, |v78|, v62, v194\n"
      "v_mfma_f32_16x16x32_f16 v[68:71], a[16:19], v[96:99], v[68:71]\n"
      "s_mul_i32 s62, s62, 272\n"
      "v_fma_f32 v194, |v79|, v63, v194\n"
      "v_mfma_f32_16x16x32_f16 v[72:75], a[32:35], v[96:99], v[8:11]\n"
      "v_pk_mul_f16 v84, v116, v132\n"
      "v_mul_f32_e64 v195, |v64|, v32\n"
      "v_mfma_f32_16x16x32_f16 v[72:75], a[36:39], v[100:103], v[72:75]\n"
      "v_permlane32_swap_b32 v192, v194\n"
      "v_fma_f32 v195, |v65|, v33, v195\n"
      "v_mfma_f32_16x16x32_f16 v[72:75], a[40:43], v[104:107], v[72:75]\n"
      "v_pk_mul_f16 v85, v117, v133\n"
      "v_fma_f32 v195, |v66|, v34, v195\n"
      "v_mfma_f32_16x16x32_f16 v[72:75], a[44:47], v[108:111], v[72:75]\n"
      "v_add_f32 v196, v192, v194\n"
      "v_fma_f32 v195, |v67|, v35, v195\n"
      "v_mfma_f32_16x16x32_f16 v[76:79], a[60:63], v[108:111], v[12:15]\n"
      "v_pk_mul_f16 v86, v118, v134\n"
      "v_fma_f32 v195, |v68|, v36, v195\n"
      "v_mfma_f32_16x16x32_f16 v[76:79], a[56:59], v[104:107], v[76:79]\n"
      "v_fma_f32 v195, |v69|, v37, v195\n"
      "v_mfma_f32_16x16x32_f16 v[76:79], a[52:55], v[100:103], v[76:79]\n"
      "v_pk_mul_f16 v87, v119, v135\n"
      "v_fma_f32 v195, |v70|, v38, v195\n"
      "v_mfma_f32_16x16x32_f16 v[76:79], a[48:51], v[96:99], v[76:79]\n"
      "v_fma_f32 v195, |v71|, v39, v195\n"
      "v_mfma_f32_16x16x32_f16 v[64:67], a[64:67], v[96:99], v[16:19]\n"
      "v_pk_mul_f16 v88, v120, v136\n"
      "v_fma_f32 v195, |v72|, v40, v195\n"
      "v_mfma_f32_16x16x32_f16 v[64:67], a[68:71], v[100:103], v[64:67]\n"
      "v_fma_f32 v195, |v73|, v41, v195\n"
      "v_mfma_f32_16x16x32_f16 v[64:67], a[72:75], v[104:107], v[64:67]\n"
      "v_pk_mul_f16 v89, v121, v137\n"
      "v_fma_f32 v195, |v74|, v42, v195\n"
      "v_mfma_f32_16x16x32_f16 v[64:67], a[76:79], v[108:111], v[64:67]\n"
      "v_fma_f32 v195, |v75|, v43, v195\n"
      "v_mfma_f32_16x16x32_f16 v[68:71], a[92:95], v[108:111], v[20:23]\n"
      "v_pk_mul_f16 v90, v122, v138\n"
      "v_fma_f32 v195, |v76|, v44, v195\n"
      "v_mfma_f32_16x16x32_f16 v[68:71], a[88:91], v[104:107], v[68:71]\n"
      "v_fma_f32 v195, |v77|, v45, v195\n"
      "v_mfma_f32_16x16x32_f16 v[68:71], a[84:87], v[100:103], v[68:71]\n"
      "v_pk_mul_f16 v91, v123, v139\n"
      "v_fma_f32 v195, |v78|, v46, v195\n"
      "v_mfma_f32_16x16x32_f16 v[68:71], a[80:83], v[96:99], v[68:71]\n"
      "v_fma_f32 v195, |v79|, v47, v195\n"
      "v_mfma_f32_16x16x32_f16 v[72:75], a[96:99], v[96:99], v[24:27]\n"
      "v_pk_mul_f16 v92, v124, v140\n"
      "v_fma_f32 v195, |v64|, v48, v195\n"
      "v_mfma_f32_16x16x32_f16 v[72:75], a[100:103], v[100:103], v[72:75]\n"
      "v_fma_f32 v195, |v65|, v49, v195\n"
      "v_mfma_f32_16x16x32_f16 v[72:75], a[104:107], v[104:107], v[72:75]\n"
      "v_pk_mul_f16 v93, v125, v141\n"
      "v_fma_f32 v195, |v66|, v50, v195\n"
      "v_mfma_f32_16x16x32_f16 v[72:75], a[108:111], v[108:111], v[72:75]\n"
      "v_fma_f32 v195, |v67|, v51, v195\n"
      "v_mfma_f32_16x16x32_f16 v[76:79], a[124:127], v[108:111], v[28:31]\n"
      "v_pk_mul_f16 v94, v126, v142\n"
      "v_fma_f32 v195, |v68|, v52, v195\n"
      "v_mfma_f32_16x16x32_f16 v[76:79], a[120:123], v[104:107], v[76:79]\n"
      "v_fma_f32 v195, |v69|, v53, v195\n"
      "v_mfma_f32_16x16x32_f16 v[76:79], a[116:119], v[100:103], v[76:79]\n"
      "v_pk_mul_f16 v95, v127, v143\n"
      "v_fma_f32 v195, |v70|, v54, v195\n"
      "v_mfma_f32_16x16x32_f16 v[76:79], a[112:115], v[96:99], v[76:79]\n"
      "v_fma_f32 v195, |v71|, v55, v195\n"
      "v_mfma_f32_16x16x32_f16 v[64:67], a[0:3], v[80:83], v[0:3]\n"
      "v_pk_mul_f16 v96, v112, v144\n"
      "v_fma_f32 v195, |v72|, v56, v195\n"
      "v_mfma_f32_16x16x32_f16 v[64:67], a[4:7], v[84:87], v[64:67]\n"
      "v_fma_f32 v195, |v73|, v57, v195\n"
      "v_mfma_f32_16x16x32_f16 v[64:67], a[8:11], v[88:91], v[64:67]\n"
      "v_pk_mul_f16 v97, v113, v145\n"
      "v_fma_f32 v195, |v74|, v58, v195\n"
      "v_mfma_f32_16x16x32_f16 v[64:67], a[12:15], v[92:95], v[64:67]\n"
      "v_fma_f32 v195, |v75|, v59, v195\n"
      "v_mfma_f32_16x16x32_f16 v[68:71], a[28:31], v[92:95], v[4:7]\n"
      "v_pk_mul_f16 v98, v114, v146\n"
      "v_fma_f32 v195, |v76|, v60, v195\n"
      "v_mfma_f32_16x16x32_f16 v[68:71], a[24:27], v[88:91], v[68:71]\n"
      "v_fma_f32 v195, |v77|, v61, v195\n"
      "v_mfma_f32_16x16x32_f16 v[68:71], a[20:23], v[84:87], v[68:71]\n"
      "v_pk_mul_f16 v99, v115, v147\n"
      "v_fma_f32 v195, |v78|, v62, v195\n"
      "v_mfma_f32_16x16x32_f16 v[68:71], a[16:19], v[80:83], v[68:71]\n"
      "v_fma_f32 v195, |v79|, v63, v195\n"
      "v_mfma_f32_16x16x32_f16 v[72:75], a[32:35], v[80:83], v[8:11]\n"
      "v_pk_mul_f16 v100, v116, v148\n"
      "v_mul_f32_e64 v192, |v64|, v32\n"
      "v_mfma_f32_16x16x32_f16 v[72:75], a[36:39], v[84:87], v[72:75]\n"
      "v_permlane32_swap_b32 v193, v195\n"
      "v_fma_f32 v192, |v65|, v33, v192\n"
      "v_mfma_f32_16x16x32_f16 v[72:75], a[40:43], v[88:91], v[72:75]\n"
      "v_pk_mul_f16 v101, v117, v149\n"
      "v_fma_f32 v192, |v66|, v34, v192\n"
      "v_mfma_f32_16x16x32_f16 v[72:75], a[44:47], v[92:95], v[72:75]\n"
      "v_add_f32 v197, v193, v195\n"
      "v_fma_f32 v192, |v67|, v35, v192\n"
      "v_mfma_f32_16x16x32_f16 v[76:79], a[60:63], v[92:95], v[12:15]\n"
      "v_pk_mul_f16 v102, v118, v150\n"
      "v_fma_f32 v192, |v68|, v36, v192\n"
      "v_mfma_f32_16x16x32_f16 v[76:79], a[56:59], v[88:91], v[76:79]\n"
      "v_fma_f32 v192, |v69|, v37, v192\n"
      "v_mfma_f32_16x16x32_f16 v[76:79], a[52:55], v[84:87], v[76:79]\n"
      "v_pk_mul_f16 v103, v119, v151\n"
      "v_fma_f32 v192, |v70|, v38, v192\n"
      "v_mfma_f32_16x16x32_f16 v[76:79], a[48:51], v[80:83], v[76:79]\n"
      "v_permlane16_swap_b32 v196, v197\n"
      "v_fma_f32 v192, |v71|, v39, v192\n"
      "v_mfma_f32_16x16x32_f16 v[64:67], a[64:67], v[80:83], v[16:19]\n"
      "v_pk_mul_f16 v104, v120, v152\n"
      "v_fma_f32 v192, |v72|, v40, v192\n"
      "v_mfma_f32_16x16x32_f16 v[64:67], a[68:71], v[84:87], v[64:67]\n"
      "v_add_f32 v198, v196, v197\n"
      "v_fma_f32 v192, |v73|, v41, v192\n"
      "v_mfma_f32_16x16x32_f16 v[64:67], a[72:75], v[88:91], v[64:67]\n"
      "v_pk_mul_f16 v105, v121, v153\n"
      "v_fma_f32 v192, |v74|, v42, v192\n"
      "v_mfma_f32_16x16x32_f16 v[64:67], a[76:79], v[92:95], v[64:67]\n"
      "ds_write_b32 v199, v198\n"
      "v_fma_f32 v192, |v75|, v43, v192\n"
      "v_mfma_f32_16x16x32_f16 v[68:71], a[92:95], v[92:95], v[20:23]\n"
      "v_pk_mul_f16 v106, v122, v154\n"
      "v_fma_f32 v192, |v76|, v44, v192\n"
      "v_mfma_f32_16x16x32_f16 v[68:71], a[88:91], v[88:91], v[68:71]\n"
      "v_fma_f32 v192, |v77|, v45, v192\n"
      "v_mfma_f32_16x16x32_f16 v[68:71], a[84:87], v[84:87], v[68:71]\n"
      "v_pk_mul_f16 v107, v123, v155\n"
      "v_fma_f32 v192, |v78|, v46, v192\n"
      "v_mfma_f32_16x16x32_f16 v[68:71], a[80:83], v[80:83], v[68:71]\n"
      "v_fma_f32 v192, |v79|, v47, v192\n"
      "v_mfma_f32_16x16x32_f16 v[72:75], a[96:99], v[80:83], v[24:27]\n"
      "v_pk_mul_f16 v108, v124, v156\n"
      "v_fma_f32 v192, |v64|, v48, v192\n"
      "v_mfma_f32_16x16x32_f16 v[72:75], a[100:103], v[84:87], v[72:75]\n"
      "v_fma_f32 v192, |v65|, v49, v192\n"
      "v_mfma_f32_16x16x32_f16 v[72:75], a[104:107], v[88:91], v[72:75]\n"
      "v_pk_mul_f16 v109, v125, v157\n"
      "v_fma_f32 v192, |v66|, v50, v192\n"
      "v_mfma_f32_16x16x32_f16 v[72:75], a[108:111], v[92:95], v[72:75]\n"
      "v_fma_f32 v192, |v67|, v51, v192\n"
      "v_mfma_f32_16x16x32_f16 v[76:79], a[124:127], v[92:95], v[28:31]\n"
      "v_pk_mul_f16 v110, v126, v158\n"
      "v_fma_f32 v192, |v68|, v52, v192\n"
      "v_mfma_f32_16x16x32_f16 v[76:79], a[120:123], v[88:91], v[76:79]\n"
      "v_fma_f32 v192, |v69|, v53, v192\n"
      "v_mfma_f32_16x16x32_f16 v[76:79], a[116:119], v[84:87], v[76:79]\n"
      "v_pk_mul_f16 v111, v127, v159\n"
      "v_fma_f32 v192, |v70|, v54, v192\n"
      "v_mfma_f32_16x16x32_f16 v[76:79], a[112:115], v[80:83], v[76:79]\n"
      "v_fma_f32 v192, |v71|, v55, v192\n"
      "v_mfma_f32_16x16x32_f16 v[64:67], a[0:3], v[96:99], v[0:3]\n"
      "v_pk_mul_f16 v80, v128, v144\n"
      "v_fma_f32 v192, |v72|, v56, v192\n"
      "v_mfma_f32_16x16x32_f16 v[64:67], a[4:7], v[100:103], v[64:67]\n"
      "v_add_u32 v199, s63, v205\n"
      "v_fma_f32 v192, |v73|, v57, v192\n"
      "v_mfma_f32_16x16x32_f16 v[64:67], a[8:11], v[104:107], v[64:67]\n"
      "v_pk_mul_f16 v81, v129, v145\n"
      "v_fma_f32 v192, |v74|, v58, v192\n"
      "v_mfma_f32_16x16x32_f16 v[64:67], a[12:15], v[108:111], v[64:67]\n"
      "v_fma_f32 v192, |v75|, v59, v192\n"
      "v_mfma_f32_16x16x32_f16 v[68:71], a[28:31], v[108:111], v[4:7]\n"
      "v_pk_mul_f16 v82, v130, v146\n"
      "v_fma_f32 v192, |v76|, v60, v192\n"
      "v_mfma_f32_16x16x32_f16 v[68:71], a[24:27], v[104:107], v[68:71]\n"
      "v_fma_f32 v192, |v77|, v61, v192\n"
      "v_mfma_f32_16x16x32_f16 v[68:71], a[20:23], v[100:103], v[68:71]\n"
      "v_pk_mul_f16 v83, v131, v147\n"
      "v_fma_f32 v192, |v78|, v62, v192\n"
      "v_mfma_f32_16x16x32_f16 v[68:71], a[16:19], v[96:99], v[68:71]\n"
      "v_fma_f32 v192, |v79|, v63, v192\n"
      "v_mfma_f32_16x16x32_f16 v[72:75], a[32:35], v[96:99], v[8:11]\n"
      "v_pk_mul_f16 v84, v132, v148\n"
      "v_mul_f32_e64 v193, |v64|, v32\n"
      "v_mfma_f32_16x16x32_f16 v[72:75], a[36:39], v[100:103], v[72:75]\n"
      "v_fma_f32 v193, |v65|, v33, v193\n"
      "v_mfma_f32_16x16x32_f16 v[72:75], a[40:43], v[104:107], v[72:75]\n"
      "v_pk_mul_f16 v85, v133, v149\n"
      "v_fma_f32 v193, |v66|, v34, v193\n"
      "v_mfma_f32_16x16x32_f16 v[72:75], a[44:47], v[108:111], v[72:75]\n"
      "v_fma_f32 v193, |v67|, v35, v193\n"
      "v_mfma_f32_16x16x32_f16 v[76:79], a[60:63], v[108:111], v[12:15]\n"
      "v_pk_mul_f16 v86, v134, v150\n"
      "v_fma_f32 v193, |v68|, v36, v193\n"
      "v_mfma_f32_16x16x32_f16 v[76:79], a[56:59], v[104:107], v[76:79]\n"
      "v_fma_f32 v193, |v69|, v37, v193\n"
      "v_mfma_f32_16x16x32_f16 v[76:79], a[52:55], v[100:103], v[76:79]\n"
      "v_pk_mul_f16 v87, v135, v151\n"
      "v_fma_f32 v193, |v70|, v38, v193\n"
      "v_mfma_f32_16x16x32_f16 v[76:79], a[48:51], v[96:99], v[76:79]\n"
      "v_fma_f32 v193, |v71|, v39, v193\n"
      "v_mfma_f32_16x16x32_f16 v[64:67], a[64:67], v[96:99], v[16:19]\n"
      "v_pk_mul_f16 v88, v136, v152\n"
      "v_fma_f32 v193, |v72|, v40, v193\n"
      "v_mfma_f32_16x16x32_f16 v[64:67], a[68:71], v[100:103], v[64:67]\n"
      "v_fma_f32 v193, |v73|, v41, v193\n"
      "v_mfma_f32_16x16x32_f16 v[64:67], a[72:75], v[104:107], v[64:67]\n"
      "v_pk_mul_f16 v89, v137, v153\n"
      "v_fma_f32 v193, |v74|, v42, v193\n"
      "v_mfma_f32_16x16x32_f16 v[64:67], a[76:79], v[108:111], v[64:67]\n"
      "v_fma_f32 v193, |v75|, v43, v193\n"
      "v_mfma_f32_16x16x32_f16 v[68:71], a[92:95], v[108:111], v[20:23]\n"
      "v_pk_mul_f16 v90, v138, v154\n"
      "v_fma_f32 v193, |v76|, v44, v193\n"
      "v_mfma_f32_16x16x32_f16 v[68:71], a[88:91], v[104:107], v[68:71]\n"
      "v_fma_f32 v193, |v77|, v45, v193\n"
      "v_mfma_f32_16x16x32_f16 v[68:71], a[84:87], v[100:103], v[68:71]\n"
      "v_pk_mul_f16 v91, v139, v155\n"
      "v_fma_f32 v193, |v78|, v46, v193\n"
      "v_mfma_f32_16x16x32_f16 v[68:71], a[80:83], v[96:99], v[68:71]\n"
      "v_fma_f32 v193, |v79|, v47, v193\n"
      "v_mfma_f32_16x16x32_f16 v[72:75], a[96:99], v[96:99], v[24:27]\n"
      "v_pk_mul_f16 v92, v140, v156\n"
      "v_fma_f32 v193, |v64|, v48, v193\n"
      "v_mfma_f32_16x16x32_f16 v[72:75], a[100:103], v[100:103], v[72:75]\n"
      "v_fma_f32 v193, |v65|, v49, v193\n"
      "v_mfma_f32_16x16x32_f16 v[72:75], a[104:107], v[104:107], v[72:75]\n"
      "v_pk_mul_f16 v93, v141, v157\n"
      "v_fma_f32 v193, |v66|, v50, v193\n"
      "v_mfma_f32_16x16x32_f16 v[72:75], a[108:111], v[108:111], v[72:75]\n"
      "v_fma_f32 v193, |v67|, v51, v193\n"
      "v_mfma_f32_16x16x32_f16 v[76:79], a[124:127], v[108:111], v[28:31]\n"
      "v_pk_mul_f16 v94, v142, v158\n"
      "v_fma_f32 v193, |v68|, v52, v193\n"
      "v_mfma_f32_16x16x32_f16 v[76:79], a[120:123], v[104:107], v[76:79]\n"
      "v_fma_f32 v193, |v69|, v53, v193\n"
      "v_mfma_f32_16x16x32_f16 v[76:79], a[116:119], v[100:103], v[76:79]\n"
      "v_pk_mul_f16 v95, v143, v159\n"
      "v_fma_f32 v193, |v70|, v54, v193\n"
      "v_mfma_f32_16x16x32_f16 v[76:79], a[112:115], v[96:99], v[76:79]\n"
      "v_fma_f32 v193, |v71|, v55, v193\n"
      "v_mfma_f32_16x16x32_f16 v[64:67], a[0:3], v[80:83], v[0:3]\n"
      "v_pk_mul_f16 v96, v128, v160\n"
      "v_fma_f32 v193, |v72|, v56, v193\n"
      "v_mfma_f32_16x16x32_f16 v[64:67], a[4:7], v[84:87], v[64:67]\n"
      "v_fma_f32 v193, |v73|, v57, v193\n"
      "v_mfma_f32_16x16x32_f16 v[64:67], a[8:11], v[88:91], v[64:67]\n"
      "v_pk_mul_f16 v97, v129, v161\n"
      "v_fma_f32 v193, |v74|, v58, v193\n"
      "v_mfma_f32_16x16x32_f16 v[64:67], a[12:15], v[92:95], v[64:67]\n"
      "v_fma_f32 v193, |v75|, v59, v193\n"
      "v_mfma_f32_16x16x32_f16 v[68:71], a[28:31], v[92:95], v[4:7]\n"
      "v_pk_mul_f16 v98, v130, v162\n"
      "v_fma_f32 v193, |v76|, v60, v193\n"
      "v_mfma_f32_16x16x32_f16 v[68:71], a[24:27], v[88:91], v[68:71]\n"
      "v_fma_f32 v193, |v77|, v61, v193\n"
      "v_mfma_f32_16x16x32_f16 v[68:71], a[20:23], v[84:87], v[68:71]\n"
      "v_pk_mul_f16 v99, v131, v163\n"
      "v_fma_f32 v193, |v78|, v62, v193\n"
      "v_mfma_f32_16x16x32_f16 v[68:71], a[16:19], v[80:83], v[68:71]\n"
      "v_fma_f32 v193, |v79|, v63, v193\n"
      "v_mfma_f32_16x16x32_f16 v[72:75], a[32:35], v[80:83], v[8:11]\n"
      "v_pk_mul_f16 v100, v132, v164\n"
      "v_mul_f32_e64 v194, |v64|, v32\n"
      "v_mfma_f32_16x16x32_f16 v[72:75], a[36:39], v[84:87], v[72:75]\n"
      "v_fma_f32 v194, |v65|, v33, v194\n"
      "v_mfma_f32_16x16x32_f16 v[72:75], a[40:43], v[88:91], v[72:75]\n"
      "v_pk_mul_f16 v101, v133, v165\n"
      "v_fma_f32 v194, |v66|, v34, v194\n"
      "v_mfma_f32_16x16x32_f16 v[72:75], a[44:47], v[92:95], v[72:75]\n"
      "v_fma_f32 v194, |v67|, v35, v194\n"
      "v_mfma_f32_16x16x32_f16 v[76:79], a[60:63], v[92:95], v[12:15]\n"
      "v_pk_mul_f16 v102, v134, v166\n"
      "v_fma_f32 v194, |v68|, v36, v194\n"
      "v_mfma_f32_16x16x32_f16 v[76:79], a[56:59], v[88:91], v[76:79]\n"
      "v_fma_f32 v194, |v69|, v37, v194\n"
      "v_mfma_f32_16x16x32_f16 v[76:79], a[52:55], v[84:87], v[76:79]\n"
      "v_pk_mul_f16 v103, v135, v167\n"
      "v_fma_f32 v194, |v70|, v38, v194\n"
      "v_mfma_f32_16x16x32_f16 v[76:79], a[48:51], v[80:83], v[76:79]\n"
      "v_fma_f32 v194, |v71|, v39, v194\n"
      "v_mfma_f32_16x16x32_f16 v[64:67], a[64:67], v[80:83], v[16:19]\n"
      "v_pk_mul_f16 v104, v136, v168\n"
      "v_fma_f32 v194, |v72|, v40, v194\n"
      "v_mfma_f32_16x16x32_f16 v[64:67], a[68:71], v[84:87], v[64:67]\n"
      "v_fma_f32 v194, |v73|, v41, v194\n"
      "v_mfma_f32_16x16x32_f16 v[64:67], a[72:75], v[88:91], v[64:67]\n"
      "v_pk_mul_f16 v105, v137, v169\n"
      "v_fma_f32 v194, |v74|, v42, v194\n"
      "v_mfma_f32_16x16x32_f16 v[64:67], a[76:79], v[92:95], v[64:67]\n"
      "v_fma_f32 v194, |v75|, v43, v194\n"
      "v_mfma_f32_16x16x32_f16 v[68:71], a[92:95], v[92:95], v[20:23]\n"
      "v_pk_mul_f16 v106, v138, v170\n"
      "v_fma_f32 v194, |v76|, v44, v194\n"
      "v_mfma_f32_16x16x32_f16 v[68:71], a[88:91], v[88:91], v[68:71]\n"
      "v_fma_f32 v194, |v77|, v45, v194\n"
      "v_mfma_f32_16x16x32_f16 v[68:71], a[84:87], v[84:87], v[68:71]\n"
      "v_pk_mul_f16 v107, v139, v171\n"
      "v_fma_f32 v194, |v78|, v46, v194\n"
      "v_mfma_f32_16x16x32_f16 v[68:71], a[80:83], v[80:83], v[68:71]\n"
      "v_fma_f32 v194, |v79|, v47, v194\n"
      "v_mfma_f32_16x16x32_f16 v[72:75], a[96:99], v[80:83], v[24:27]\n"
      "v_pk_mul_f16 v108, v140, v172\n"
      "v_fma_f32 v194, |v64|, v48, v194\n"
      "v_mfma_f32_16x16x32_f16 v[72:75], a[100:103], v[84:87], v[72:75]\n"
      "v_fma_f32 v194, |v65|, v49, v194\n"
      "v_mfma_f32_16x16x32_f16 v[72:75], a[104:107], v[88:91], v[72:75]\n"
      "v_pk_mul_f16 v109, v141, v173\n"
      "v_fma_f32 v194, |v66|, v50, v194\n"
      "v_mfma_f32_16x16x32_f16 v[72:75], a[108:111], v[92:95], v[72:75]\n"
      "v_fma_f32 v194, |v67|, v51, v194\n"
      "v_mfma_f32_16x16x32_f16 v[76:79], a[124:127], v[92:95], v[28:31]\n"
      "v_pk_mul_f16 v110, v142, v174\n"
      "v_fma_f32 v194, |v68|, v52, v194\n"
      "v_mfma_f32_16x16x32_f16 v[76:79], a[120:123], v[88:91], v[76:79]\n"
      "s_and_b32 s52, s60, 0x1f00000\n"
      "v_fma_f32 v194, |v69|, v53, v194\n"
      "v_mfma_f32_16x16x32_f16 v[76:79], a[116:119], v[84:87], v[76:79]\n"
      "v_pk_mul_f16 v111, v143, v175\n"
      "v_fma_f32 v194, |v70|, v54, v194\n"
      "v_mfma_f32_16x16x32_f16 v[76:79], a[112:115], v[80:83], v[76:79]\n"
      "v_fma_f32 v194, |v71|, v55, v194\n"
      "s_branch .Lmid_%=\n"
      ".Lmidspecial_%=:\n"
      "s_bitcmp1_b32 s60, 21\n"
      "s_cbranch_scc0 .Lnost1_%=\n"
      "s_waitcnt vmcnt(7)\n"
      "v_lshlrev_b32 v176, 16, v210\n"
      "v_and_b32 v177, s69, v210\n"
      "v_lshlrev_b32 v178, 16, v211\n"
      "v_and_b32 v179, s69, v211\n"
      "v_lshlrev_b32 v180, 16, v212\n"
      "v_and_b32 v181, s69, v212\n"
      "v_lshlrev_b32 v182, 16, v213\n"
      "v_and_b32 v183, s69, v213\n"
      "v_pk_mul_f32 v[176:177], v[176:177], v[208:209]\n"
      "v_pk_mul_f32 v[178:179], v[178:179], v[208:209]\n"
      "v_pk_mul_f32 v[180:181], v[180:181], v[208:209]\n"
      "v_pk_mul_f32 v[182:183], v[182:183], v[208:209]\n"
      "v_cvt_pk_f16_f32 v210, v176, v177\n"
      "v_cvt_pk_f16_f32 v211, v178, v179\n"
      "v_cvt_pk_f16_f32 v212, v180, v181\n"
      "v_cvt_pk_f16_f32 v213, v182, v183\n"
      "ds_write_b128 v206, v[210:213] offset:2176\n"
      "s_waitcnt vmcnt(6)\n"
      "v_lshlrev_b32 v184, 16, v214\n"
      "v_and_b32 v185, s69, v214\n"
      "v_lshlrev_b32 v186, 16, v215\n"
      "v_and_b32 v187, s69, v215\n"
      "v_lshlrev_b32 v188, 16, v216\n"
      "v_and_b32 v189, s69, v216\n"
      "v_lshlrev_b32 v190, 16, v217\n"
      "v_and_b32 v191, s69, v217\n"
      "v_pk_mul_f32 v[184:185], v[184:185], v[208:209]\n"
      "v_pk_mul_f32 v[186:187], v[186:187], v[208:209]\n"
      "v_pk_mul_f32 v[188:189], v[188:189], v[208:209]\n"
      "v_pk_mul_f32 v[190:191], v[190:191], v[208:209]\n"
      "v_cvt_pk_f16_f32 v214, v184, v185\n"
      "v_cvt_pk_f16_f32 v215, v186, v187\n"
      "v_cvt_pk_f16_f32 v216, v188, v189\n"
      "v_cvt_pk_f16_f32 v217, v190, v191\n"
      "ds_write_b128 v206, v[214:217] offset:2448\n"
      "s_waitcnt vmcnt(5)\n"
      "v_lshlrev_b32 v176, 16, v218\n"
      "v_and_b32 v177, s69, v218\n"
      "v_lshlrev_b32 v178, 16, v219\n"
      "v_and_b32 v179, s69, v219\n"
      "v_lshlrev_b32 v180, 16, v220\n"
      "v_and_b32 v181, s69, v220\n"
      "v_lshlrev_b32 v182, 16, v221\n"
      "v_and_b32 v183, s69, v221\n"
      "v_pk_mul_f32 v[176:177], v[176:177], v[208:209]\n"
      "v_pk_mul_f32 v[178:179], v[178:179], v[208:209]\n"
      "v_pk_mul_f32 v[180:181], v[180:181], v[208:209]\n"
      "v_pk_mul_f32 v[182:183], v[182:183], v[208:209]\n"
      "v_cvt_pk_f16_f32 v218, v176, v177\n"
      "v_cvt_pk_f16_f32 v219, v178, v179\n"
      "v_cvt_pk_f16_f32 v220, v180, v181\n"
      "v_cvt_pk_f16_f32 v221, v182, v183\n"
      "ds_write_b128 v206, v[218:221] offset:2720\n"
      "s_waitcnt vmcnt(4)\n"
      "v_lshlrev_b32 v184, 16, v222\n"
      "v_and_b32 v185, s69, v222\n"
      "v_lshlrev_b32 v186, 16, v223\n"
      "v_and_b32 v187, s69, v223\n"
      "v_lshlrev_b32 v188, 16, v224\n"
      "v_and_b32 v189, s69, v224\n"
      "v_lshlrev_b32 v190, 16, v225\n"
      "v_and_b32 v191, s69, v225\n"
      "v_pk_mul_f32 v[184:185], v[184:185], v[208:209]\n"
      "v_pk_mul_f32 v[186:187], v[186:187], v[208:209]\n"
      "v_pk_mul_f32 v[188:189], v[188:189], v[208:209]\n"
      "v_pk_mul_f32 v[190:191], v[190:191], v[208:209]\n"
      "v_cvt_pk_f16_f32 v222, v184, v185\n"
      "v_cvt_pk_f16_f32 v223, v186, v187\n"
      "v_cvt_pk_f16_f32 v224, v188, v189\n"
      "v_cvt_pk_f16_f32 v225, v190, v191\n"
      "ds_write_b128 v206, v[222:225] offset:2992\n"
      "s_waitcnt vmcnt(3)\n"
      "v_lshlrev_b32 v176, 16, v226\n"
      "v_and_b32 v177, s69, v226\n"
      "v_lshlrev_b32 v178, 16, v227\n"
      "v_and_b32 v179, s69, v227\n"
      "v_lshlrev_b32 v180, 16, v228\n"
      "v_and_b32 v181, s69, v228\n"
      "v_lshlrev_b32 v182, 16, v229\n"
      "v_and_b32 v183, s69, v229\n"
      "v_pk_mul_f32 v[176:177], v[176:177], v[208:209]\n"
      "v_pk_mul_f32 v[178:179], v[178:179], v[208:209]\n"
      "v_pk_mul_f32 v[180:181], v[180:181], v[208:209]\n"
      "v_pk_mul_f32 v[182:183], v[182:183], v[208:209]\n"
      "v_cvt_pk_f16_f32 v226, v176, v177\n"
      "v_cvt_pk_f16_f32 v227, v178, v179\n"
      "v_cvt_pk_f16_f32 v228, v180, v181\n"
      "v_cvt_pk_f16_f32 v229, v182, v183\n"
      "ds_write_b128 v206, v[226:229] offset:3264\n"
      "s_waitcnt vmcnt(2)\n"
      "v_lshlrev_b32 v184, 16, v230\n"
      "v_and_b32 v185, s69, v230\n"
      "v_lshlrev_b32 v186, 16, v231\n"
      "v_and_b32 v187, s69, v231\n"
      "v_lshlrev_b32 v188, 16, v232\n"
      "v_and_b32 v189, s69, v232\n"
      "v_lshlrev_b32 v190, 16, v233\n"
      "v_and_b32 v191, s69, v233\n"
      "v_pk_mul_f32 v[184:185], v[184:185], v[208:209]\n"
      "v_pk_mul_f32 v[186:187], v[186:187], v[208:209]\n"
      "v_pk_mul_f32 v[188:189], v[188:189], v[208:209]\n"
      "v_pk_mul_f32 v[190:191], v[190:191], v[208:209]\n"
      "v_cvt_pk_f16_f32 v230, v184, v185\n"
      "v_cvt_pk_f16_f32 v231, v186, v187\n"
      "v_cvt_pk_f16_f32 v232, v188, v189\n"
      "v_cvt_pk_f16_f32 v233, v190, v191\n"
      "ds_write_b128 v206, v[230:233] offset:3536\n"
      "s_waitcnt vmcnt(1)\n"
      "v_lshlrev_b32 v176, 16, v234\n"
      "v_and_b32 v177, s69, v234\n"
      "v_lshlrev_b32 v178, 16, v235\n"
      "v_and_b32 v179, s69, v235\n"
      "v_lshlrev_b32 v180, 16, v236\n"
      "v_and_b32 v181, s69, v236\n"
      "v_lshlrev_b32 v182, 16, v237\n"
      "v_and_b32 v183, s69, v237\n"
      "v_pk_mul_f32 v[176:177], v[176:177], v[208:209]\n"
      "v_pk_mul_f32 v[178:179], v[178:179], v[208:209]\n"
      "v_pk_mul_f32 v[180:181], v[180:181], v[208:209]\n"
      "v_pk_mul_f32 v[182:183], v[182:183], v[208:209]\n"
      "v_cvt_pk_f16_f32 v234, v176, v177\n"
      "v_cvt_pk_f16_f32 v235, v178, v179\n"
      "v_cvt_pk_f16_f32 v236, v180, v181\n"
      "v_cvt_pk_f16_f32 v237, v182, v183\n"
      "ds_write_b128 v206, v[234:237] offset:3808\n"
      "s_waitcnt vmcnt(0)\n"
      "v_lshlrev_b32 v184, 16, v238\n"
      "v_and_b32 v185, s69, v238\n"
      "v_lshlrev_b32 v186, 16, v239\n"
      "v_and_b32 v187, s69, v239\n"
      "v_lshlrev_b32 v188, 16, v240\n"
      "v_and_b32 v189, s69, v240\n"
      "v_lshlrev_b32 v190, 16, v241\n"
      "v_and_b32 v191, s69, v241\n"
      "v_pk_mul_f32 v[184:185], v[184:185], v[208:209]\n"
      "v_pk_mul_f32 v[186:187], v[186:187], v[208:209]\n"
      "v_pk_mul_f32 v[188:189], v[188:189], v[208:209]\n"
      "v_pk_mul_f32 v[190:191], v[190:191], v[208:209]\n"
      "v_cvt_pk_f16_f32 v238, v184, v185\n"
      "v_cvt_pk_f16_f32 v239, v186, v187\n"
      "v_cvt_pk_f16_f32 v240, v188, v189\n"
      "v_cvt_pk_f16_f32 v241, v190, v191\n"
      "ds_write_b128 v206, v[238:241] offset:4080\n"
      "v_mov_b32 v242, 160704\n"
      "v_mov_b32 v243, 1\n"
      "s_mov_b64 s[98:99], exec\n"
      "s_mov_b64 exec, 1\n"
      "ds_add_u32 v242, v243\n"
      "s_mov_b64 exec, s[98:99]\n"
      "v_accvgpr_read_b32 v176, a128\n"
      "v_accvgpr_read_b32 v177, a132\n"
      "v_accvgpr_read_b32 v178, a136\n"
      "v_accvgpr_read_b32 v179, a140\n"
      "v_accvgpr_read_b32 v180, a144\n"
      "v_accvgpr_read_b32 v181, a148\n"
      "v_accvgpr_read_b32 v182, a152\n"
      "v_accvgpr_read_b32 v183, a156\n"
      "v_accvgpr_read_b32 v184, a160\n"
      "v_accvgpr_read_b32 v185, a164\n"
      "global_load_dwordx4 a[128:131], v176, s[38:39]\n"
      "global_load_dwordx4 a[132:135], v177, s[38:39]\n"
      "global_load_dwordx4 a[136:139], v178, s[38:39]\n"
      "global_load_dwordx4 a[140:143], v179, s[38:39]\n"
      "global_load_dwordx4 a[144:147], v180, s[38:39]\n"
      "global_load_dwordx4 a[148:151], v181, s[38:39]\n"
      "global_load_dwordx4 a[152:155], v182, s[38:39]\n"
      "global_load_dwordx4 a[156:159], v183, s[38:39]\n"
      "global_load_dwordx4 a[160:163], v184, s[38:39]\n"
      "global_load_dwordx4 a[164:167], v185, s[38:39]\n"
      ".Lnost1_%=:\n"
      "s_bitcmp1_b32 s60, 22\n"
      "s_cbranch_scc0 .Lnost2_%=\n"
      "s_waitcnt vmcnt(9)\n"
      "v_accvgpr_read_b32 v210, a128\n"
      "v_accvgpr_read_b32 v211, a129\n"
      "v_accvgpr_read_b32 v212, a130\n"
      "v_accvgpr_read_b32 v213, a131\n"
      "v_lshlrev_b32 v176, 16, v210\n"
      "v_and_b32 v177, s69, v210\n"
      "v_lshlrev_b32 v178, 16, v211\n"
      "v_and_b32 v179, s69, v211\n"
      "v_lshlrev_b32 v180, 16, v212\n"
      "v_and_b32 v181, s69, v212\n"
      "v_lshlrev_b32 v182, 16, v213\n"
      "v_and_b32 v183, s69, v213\n"
      "v_pk_mul_f32 v[176:177], v[176:177], v[208:209]\n"
      "v_pk_mul_f32 v[178:179], v[178:179], v[208:209]\n"
      "v_pk_mul_f32 v[180:181], v[180:181], v[208:209]\n"
      "v_pk_mul_f32 v[182:183], v[182:183], v[208:209]\n"
      "v_cvt_pk_f16_f32 v210, v176, v177\n"
      "v_cvt_pk_f16_f32 v211, v178, v179\n"
      "v_cvt_pk_f16_f32 v212, v180, v181\n"
      "v_cvt_pk_f16_f32 v213, v182, v183\n"
      "ds_write_b128 v206, v[210:213] offset:4352\n"
      "s_waitcnt vmcnt(8)\n"
      "v_accvgpr_read_b32 v214, a132\n"
      "v_accvgpr_read_b32 v215, a133\n"
      "v_accvgpr_read_b32 v216, a134\n"
      "v_accvgpr_read_b32 v217, a135\n"
      "v_lshlrev_b32 v184, 16, v214\n"
      "v_and_b32 v185, s69, v214\n"
      "v_lshlrev_b32 v186, 16, v215\n"
      "v_and_b32 v187, s69, v215\n"
      "v_lshlrev_b32 v188, 16, v216\n"
      "v_and_b32 v189, s69, v216\n"
      "v_lshlrev_b32 v190, 16, v217\n"
      "v_and_b32 v191, s69, v217\n"
      "v_pk_mul_f32 v[184:185], v[184:185], v[208:209]\n"
      "v_pk_mul_f32 v[186:187], v[186:187], v[208:209]\n"
      "v_pk_mul_f32 v[188:189], v[188:189], v[208:209]\n"
      "v_pk_mul_f32 v[190:191], v[190:191], v[208:209]\n"
      "v_cvt_pk_f16_f32 v214, v184, v185\n"
      "v_cvt_pk_f16_f32 v215, v186, v187\n"
      "v_cvt_pk_f16_f32 v216, v188, v189\n"
      "v_cvt_pk_f16_f32 v217, v190, v191\n"
      "ds_write_b128 v206, v[214:217] offset:4624\n"
      "s_waitcnt vmcnt(7)\n"
      "v_accvgpr_read_b32 v218, a136\n"
      "v_accvgpr_read_b32 v219, a137\n"
      "v_accvgpr_read_b32 v220, a138\n"
      "v_accvgpr_read_b32 v221, a139\n"
      "v_lshlrev_b32 v176, 16, v218\n"
      "v_and_b32 v177, s69, v218\n"
      "v_lshlrev_b32 v178, 16, v219\n"
      "v_and_b32 v179, s69, v219\n"
      "v_lshlrev_b32 v180, 16, v220\n"
      "v_and_b32 v181, s69, v220\n"
      "v_lshlrev_b32 v182, 16, v221\n"
      "v_and_b32 v183, s69, v221\n"
      "v_pk_mul_f32 v[176:177], v[176:177], v[208:209]\n"
      "v_pk_mul_f32 v[178:179], v[178:179], v[208:209]\n"
      "v_pk_mul_f32 v[180:181], v[180:181], v[208:209]\n"
      "v_pk_mul_f32 v[182:183], v[182:183], v[208:209]\n"
      "v_cvt_pk_f16_f32 v218, v176, v177\n"
      "v_cvt_pk_f16_f32 v219, v178, v179\n"
      "v_cvt_pk_f16_f32 v220, v180, v181\n"
      "v_cvt_pk_f16_f32 v221, v182, v183\n"
      "ds_write_b128 v206, v[218:221] offset:4896\n"
      "s_waitcnt vmcnt(6)\n"
      "v_accvgpr_read_b32 v222, a140\n"
      "v_accvgpr_read_b32 v223, a141\n"
      "v_accvgpr_read_b32 v224, a142\n"
      "v_accvgpr_read_b32 v225, a143\n"
      "v_lshlrev_b32 v184, 16, v222\n"
      "v_and_b32 v185, s69, v222\n"
      "v_lshlrev_b32 v186, 16, v223\n"
      "v_and_b32 v187, s69, v223\n"
      "v_lshlrev_b32 v188, 16, v224\n"
      "v_and_b32 v189, s69, v224\n"
      "v_lshlrev_b32 v190, 16, v225\n"
      "v_and_b32 v191, s69, v225\n"
      "v_pk_mul_f32 v[184:185], v[184:185], v[208:209]\n"
      "v_pk_mul_f32 v[186:187], v[186:187], v[208:209]\n"
      "v_pk_mul_f32 v[188:189], v[188:189], v[208:209]\n"
      "v_pk_mul_f32 v[190:191], v[190:191], v[208:209]\n"
      "v_cvt_pk_f16_f32 v222, v184, v185\n"
      "v_cvt_pk_f16_f32 v223, v186, v187\n"
      "v_cvt_pk_f16_f32 v224, v188, v189\n"
      "v_cvt_pk_f16_f32 v225, v190, v191\n"
      "ds_write_b128 v206, v[222:225] offset:5168\n"
      "s_waitcnt vmcnt(5)\n"
      "v_accvgpr_read_b32 v210, a144\n"
      "v_accvgpr_read_b32 v211, a145\n"
      "v_accvgpr_read_b32 v212, a146\n"
      "v_accvgpr_read_b32 v213, a147\n"
      "v_lshlrev_b32 v176, 16, v210\n"
      "v_and_b32 v177, s69, v210\n"
      "v_lshlrev_b32 v178, 16, v211\n"
      "v_and_b32 v179, s69, v211\n"
      "v_lshlrev_b32 v180, 16, v212\n"
      "v_and_b32 v181, s69, v212\n"
      "v_lshlrev_b32 v182, 16, v213\n"
      "v_and_b32 v183, s69, v213\n"
      "v_pk_mul_f32 v[176:177], v[176:177], v[208:209]\n"
      "v_pk_mul_f32 v[178:179], v[178:179], v[208:209]\n"
      "v_pk_mul_f32 v[180:181], v[180:181], v[208:209]\n"
      "v_pk_mul_f32 v[182:183], v[182:183], v[208:209]\n"
      "v_cvt_pk_f16_f32 v210, v176, v177\n"
      "v_cvt_pk_f16_f32 v211, v178, v179\n"
      "v_cvt_pk_f16_f32 v212, v180, v181\n"
      "v_cvt_pk_f16_f32 v213, v182, v183\n"
      "ds_write_b128 v206, v[210:213] offset:5440\n"
      "s_waitcnt vmcnt(4)\n"
      "v_accvgpr_read_b32 v214, a148\n"
      "v_accvgpr_read_b32 v215, a149\n"
      "v_accvgpr_read_b32 v216, a150\n"
      "v_accvgpr_read_b32 v217, a151\n"
      "v_lshlrev_b32 v184, 16, v214\n"
      "v_and_b32 v185, s69, v214\n"
      "v_lshlrev_b32 v186, 16, v215\n"
      "v_and_b32 v187, s69, v215\n"
      "v_lshlrev_b32 v188, 16, v216\n"
      "v_and_b32 v189, s69, v216\n"
      "v_lshlrev_b32 v190, 16, v217\n"
      "v_and_b32 v191, s69, v217\n"
      "v_pk_mul_f32 v[184:185], v[184:185], v[208:209]\n"
      "v_pk_mul_f32 v[186:187], v[186:187], v[208:209]\n"
      "v_pk_mul_f32 v[188:189], v[188:189], v[208:209]\n"
      "v_pk_mul_f32 v[190:191], v[190:191], v[208:209]\n"
      "v_cvt_pk_f16_f32 v214, v184, v185\n"
      "v_cvt_pk_f16_f32 v215, v186, v187\n"
      "v_cvt_pk_f16_f32 v216, v188, v189\n"
      "v_cvt_pk_f16_f32 v217, v190, v191\n"
      "ds_write_b128 v206, v[214:217] offset:5712\n"
      "s_waitcnt vmcnt(3)\n"
      "v_accvgpr_read_b32 v218, a152\n"
      "v_accvgpr_read_b32 v219, a153\n"
      "v_accvgpr_read_b32 v220, a154\n"
      "v_accvgpr_read_b32 v221, a155\n"
      "v_lshlrev_b32 v176, 16, v218\n"
      "v_and_b32 v177, s69, v218\n"
      "v_lshlrev_b32 v178, 16, v219\n"
      "v_and_b32 v179, s69, v219\n"
      "v_lshlrev_b32 v180, 16, v220\n"
      "v_and_b32 v181, s69, v220\n"
      "v_lshlrev_b32 v182, 16, v221\n"
      "v_and_b32 v183, s69, v221\n"
      "v_pk_mul_f32 v[176:177], v[176:177], v[208:209]\n"
      "v_pk_mul_f32 v[178:179], v[178:179], v[208:209]\n"
      "v_pk_mul_f32 v[180:181], v[180:181], v[208:209]\n"
      "v_pk_mul_f32 v[182:183], v[182:183], v[208:209]\n"
      "v_cvt_pk_f16_f32 v218, v176, v177\n"
      "v_cvt_pk_f16_f32 v219, v178, v179\n"
      "v_cvt_pk_f16_f32 v220, v180, v181\n"
      "v_cvt_pk_f16_f32 v221, v182, v183\n"
      "ds_write_b128 v206, v[218:221] offset:5984\n"
      "s_waitcnt vmcnt(2)\n"
      "v_accvgpr_read_b32 v222, a156\n"
      "v_accvgpr_read_b32 v223, a157\n"
      "v_accvgpr_read_b32 v224, a158\n"
      "v_accvgpr_read_b32 v225, a159\n"
      "v_lshlrev_b32 v184, 16, v222\n"
      "v_and_b32 v185, s69, v222\n"
      "v_lshlrev_b32 v186, 16, v223\n"
      "v_and_b32 v187, s69, v223\n"
      "v_lshlrev_b32 v188, 16, v224\n"
      "v_and_b32 v189, s69, v224\n"
      "v_lshlrev_b32 v190, 16, v225\n"
      "v_and_b32 v191, s69, v225\n"
      "v_pk_mul_f32 v[184:185], v[184:185], v[208:209]\n"
      "v_pk_mul_f32 v[186:187], v[186:187], v[208:209]\n"
      "v_pk_mul_f32 v[188:189], v[188:189], v[208:209]\n"
      "v_pk_mul_f32 v[190:191], v[190:191], v[208:209]\n"
      "v_cvt_pk_f16_f32 v222, v184, v185\n"
      "v_cvt_pk_f16_f32 v223, v186, v187\n"
      "v_cvt_pk_f16_f32 v224, v188, v189\n"
      "v_cvt_pk_f16_f32 v225, v190, v191\n"
      "ds_write_b128 v206, v[222:225] offset:6256\n"
      "s_waitcnt vmcnt(1)\n"
      "v_accvgpr_read_b32 v210, a160\n"
      "v_accvgpr_read_b32 v211, a161\n"
      "v_accvgpr_read_b32 v212, a162\n"
      "v_accvgpr_read_b32 v213, a163\n"
      "v_lshlrev_b32 v176, 16, v210\n"
      "v_and_b32 v177, s69, v210\n"
      "v_lshlrev_b32 v178, 16, v211\n"
      "v_and_b32 v179, s69, v211\n"
      "v_lshlrev_b32 v180, 16, v212\n"
      "v_and_b32 v181, s69, v212\n"
      "v_lshlrev_b32 v182, 16, v213\n"
      "v_and_b32 v183, s69, v213\n"
      "v_pk_mul_f32 v[176:177], v[176:177], v[208:209]\n"
      "v_pk_mul_f32 v[178:179], v[178:179], v[208:209]\n"
      "v_pk_mul_f32 v[180:181], v[180:181], v[208:209]\n"
      "v_pk_mul_f32 v[182:183], v[182:183], v[208:209]\n"
      "v_cvt_pk_f16_f32 v210, v176, v177\n"
      "v_cvt_pk_f16_f32 v211, v178, v179\n"
      "v_cvt_pk_f16_f32 v212, v180, v181\n"
      "v_cvt_pk_f16_f32 v213, v182, v183\n"
      "ds_write_b128 v206, v[210:213] offset:6528\n"
      "s_waitcnt vmcnt(0)\n"
      "v_accvgpr_read_b32 v214, a164\n"
      "v_accvgpr_read_b32 v215, a165\n"
      "v_accvgpr_read_b32 v216, a166\n"
      "v_accvgpr_read_b32 v217, a167\n"
      "v_lshlrev_b32 v184, 16, v214\n"
      "v_and_b32 v185, s69, v214\n"
      "v_lshlrev_b32 v186, 16, v215\n"
      "v_and_b32 v187, s69, v215\n"
      "v_lshlrev_b32 v188, 16, v216\n"
      "v_and_b32 v189, s69, v216\n"
      "v_lshlrev_b32 v190, 16, v217\n"
      "v_and_b32 v191, s69, v217\n"
      "v_pk_mul_f32 v[184:185], v[184:185], v[208:209]\n"
      "v_pk_mul_f32 v[186:187], v[186:187], v[208:209]\n"
      "v_pk_mul_f32 v[188:189], v[188:189], v[208:209]\n"
      "v_pk_mul_f32 v[190:191], v[190:191], v[208:209]\n"
      "v_cvt_pk_f16_f32 v214, v184, v185\n"
      "v_cvt_pk_f16_f32 v215, v186, v187\n"
      "v_cvt_pk_f16_f32 v216, v188, v189\n"
      "v_cvt_pk_f16_f32 v217, v190, v191\n"
      "ds_write_b128 v206, v[214:217] offset:6800\n"
      "v_mov_b32 v242, 160708\n"
      "v_mov_b32 v243, 1\n"
      "s_mov_b64 s[98:99], exec\n"
      "s_mov_b64 exec, 1\n"
      "ds_add_u32 v242, v243\n"
      "s_mov_b64 exec, s[98:99]\n"
      ".Lnost2_%=:\n"
      "s_bitcmp1_b32 s60, 23\n"
      "s_cbranch_scc0 .Lnow1_%=\n"
      "v_mov_b32 v242, 160704\n"
      "s_mov_b32 s96, 0x400000\n"
      ".Lspin1_%=:\n"
      "s_sub_u32 s96, s96, 1\n"
      "s_cbranch_scc1 .Lspin1x_%=\n"
      "ds_read_b32 v243, v242\n"
      "s_waitcnt lgkmcnt(0)\n"
      "v_readfirstlane_b32 s54, v243\n"
      "s_nop 3\n"
      "s_cmp_lt_u32 s54, 4\n"
      "s_cbranch_scc1 .Lspin1_%=\n"
      "s_branch .Lspin1y_%=\n"
      ".Lspin1x_%=:\n"
      "s_mov_b32 s51, 1\n"
      ".Lspin1y_%=:\n"
      ".Lnow1_%=:\n"
      "s_bitcmp1_b32 s60, 24\n"
      "s_cbranch_scc0 .Lnow2_%=\n"
      "v_mov_b32 v242, 160708\n"
      "s_mov_b32 s96, 0x400000\n"
      ".Lspin2_%=:\n"
      "s_sub_u32 s96, s96, 1\n"
      "s_cbranch_scc1 .Lspin2x_%=\n"
      "ds_read_b32 v243, v242\n"
      "s_waitcnt lgkmcnt(0)\n"
      "v_readfirstlane_b32 s54, v243\n"
      "s_nop 3\n"
      "s_cmp_lt_u32 s54, 4\n"
      "s_cbranch_scc1 .Lspin2_%=\n"
      "s_branch .Lspin2y_%=\n"
      ".Lspin2x_%=:\n"
      "s_mov_b32 s51, 1\n"
      ".Lspin2y_%=:\n"
      ".Lnow2_%=:\n"
      "s_bitcmp1_b32 s60, 20\n"
      "s_cbranch_scc0 .Llast_%=\n"
      "s_bfe_u32 s64, s60, 0x3000f\n"
      "s_mul_i32 s64, s64, 1088\n"
      "v_mfma_f32_16x16x32_f16 v[64:67], a[0:3], v[96:99], v[0:3]\n"
      "v_add_u32 v201, s62, v200\n"
      "v_add_u32 v202, s64, v200\n"
      "v_fma_f32 v194, |v72|, v56, v194\n"
      "v_mfma_f32_16x16x32_f16 v[64:67], a[4:7], v[100:103], v[64:67]\n"
      "ds_read_b128 v[112:115], v202 offset:0\n"
      "ds_read_b128 v[116:119], v202 offset:64\n"
      "v_fma_f32 v194, |v73|, v57, v194\n"
      "v_mfma_f32_16x16x32_f16 v[64:67], a[8:11], v[104:107], v[64:67]\n"
      "ds_read_b128 v[120:123], v202 offset:128\n"
      "ds_read_b128 v[124:127], v202 offset:192\n"
      "v_fma_f32 v194, |v74|, v58, v194\n"
      "v_mfma_f32_16x16x32_f16 v[64:67], a[12:15], v[108:111], v[64:67]\n"
      "ds_read_b128 v[176:179], v201 offset:0\n"
      "ds_read_b128 v[180:183], v201 offset:64\n"
      "v_fma_f32 v194, |v75|, v59, v194\n"
      "v_mfma_f32_16x16x32_f16 v[68:71], a[28:31], v[108:111], v[4:7]\n"
      "ds_read_b128 v[184:187], v201 offset:128\n"
      "ds_read_b128 v[188:191], v201 offset:192\n"
      "v_fma_f32 v194, |v76|, v60, v194\n"
      "v_mfma_f32_16x16x32_f16 v[68:71], a[24:27], v[104:107], v[68:71]\n"
      "ds_read_b128 v[128:131], v202 offset:272\n"
      "ds_read_b128 v[132:135], v202 offset:336\n"
      "s_add_u32 m0, m0, 1\n"
      "v_fma_f32 v194, |v77|, v61, v194\n"
      "v_mfma_f32_16x16x32_f16 v[68:71], a[20:23], v[100:103], v[68:71]\n"
      "ds_read_b128 v[136:139], v202 offset:400\n"
      "ds_read_b128 v[140:143], v202 offset:464\n"
      "s_and_b32 s53, s60, 0x2000000\n"
      "v_fma_f32 v194, |v78|, v62, v194\n"
      "v_mfma_f32_16x16x32_f16 v[68:71], a[16:19], v[96:99], v[68:71]\n"
      "ds_read_b128 v[144:147], v202 offset:544\n"
      "ds_read_b128 v[148:151], v202 offset:608\n"
      "s_movrels_b32 s61, s70\n"
      "v_fma_f32 v194, |v79|, v63, v194\n"
      "v_mfma_f32_16x16x32_f16 v[72:75], a[32:35], v[96:99], v[8:11]\n"
      "ds_read_b128 v[152:155], v202 offset:672\n"
      "ds_read_b128 v[156:159], v202 offset:736\n"
      "s_and_b32 s54, s61, 0xc0000\n"
      "v_mul_f32_e64 v195, |v64|, v32\n"
      "v_mfma_f32_16x16x32_f16 v[72:75], a[36:39], v[100:103], v[72:75]\n"
      "ds_read_b128 v[160:163], v202 offset:816\n"
      "ds_read_b128 v[164:167], v202 offset:880\n"
      "v_permlane32_swap_b32 v192, v194\n"
      "v_fma_f32 v195, |v65|, v33, v195\n"
      "v_mfma_f32_16x16x32_f16 v[72:75], a[40:43], v[104:107], v[72:75]\n"
      "ds_read_b128 v[168:171], v202 offset:944\n"
      "ds_read_b128 v[172:175], v202 offset:1008\n"
      "s_mov_b32 s60, s61\n"
      "v_fma_f32 v195, |v66|, v34, v195\n"
      "v_mfma_f32_16x16x32_f16 v[72:75], a[44:47], v[108:111], v[72:75]\n"
      "v_add_f32 v196, v192, v194\n"
      "v_fma_f32 v195, |v67|, v35, v195\n"
      "v_mfma_f32_16x16x32_f16 v[76:79], a[60:63], v[108:111], v[12:15]\n"
      "s_or_b32 s53, s53, s54\n"
      "v_fma_f32 v195, |v68|, v36, v195\n"
      "v_mfma_f32_16x16x32_f16 v[76:79], a[56:59], v[104:107], v[76:79]\n"
      "v_fma_f32 v195, |v69|, v37, v195\n"
      "v_mfma_f32_16x16x32_f16 v[76:79], a[52:55], v[100:103], v[76:79]\n"
      "s_waitcnt lgkmcnt(12)\n"
      "v_fma_f32 v195, |v70|, v38, v195\n"
      "v_mfma_f32_16x16x32_f16 v[76:79], a[48:51], v[96:99], v[76:79]\n"
      "v_fma_f32 v195, |v71|, v39, v195\n"
      "v_mfma_f32_16x16x32_f16 v[64:67], a[64:67], v[96:99], v[16:19]\n"
      "v_pk_mul_f16 v80, v112, v176\n"
      "v_fma_f32 v195, |v72|, v40, v195\n"
      "v_mfma_f32_16x16x32_f16 v[64:67], a[68:71], v[100:103], v[64:67]\n"
      "v_pk_mul_f16 v81, v113, v177\n"
      "v_fma_f32 v195, |v73|, v41, v195\n"
      "v_mfma_f32_16x16x32_f16 v[64:67], a[72:75], v[104:107], v[64:67]\n"
      "v_pk_mul_f16 v82, v114, v178\n"
      "v_fma_f32 v195, |v74|, v42, v195\n"
      "v_mfma_f32_16x16x32_f16 v[64:67], a[76:79], v[108:111], v[64:67]\n"
      "v_pk_mul_f16 v83, v115, v179\n"
      "v_fma_f32 v195, |v75|, v43, v195\n"
      "v_mfma_f32_16x16x32_f16 v[68:71], a[92:95], v[108:111], v[20:23]\n"
      "v_pk_mul_f16 v84, v116, v180\n"
      "v_fma_f32 v195, |v76|, v44, v195\n"
      "v_mfma_f32_16x16x32_f16 v[68:71], a[88:91], v[104:107], v[68:71]\n"
      "v_pk_mul_f16 v85, v117, v181\n"
      "v_fma_f32 v195, |v77|, v45, v195\n"
      "v_mfma_f32_16x16x32_f16 v[68:71], a[84:87], v[100:103], v[68:71]\n"
      "v_pk_mul_f16 v86, v118, v182\n"
      "v_fma_f32 v195, |v78|, v46, v195\n"
      "v_mfma_f32_16x16x32_f16 v[68:71], a[80:83], v[96:99], v[68:71]\n"
      "v_pk_mul_f16 v87, v119, v183\n"
      "v_fma_f32 v195, |v79|, v47, v195\n"
      "v_mfma_f32_16x16x32_f16 v[72:75], a[96:99], v[96:99], v[24:27]\n"
      "v_pk_mul_f16 v88, v120, v184\n"
      "v_fma_f32 v195, |v64|, v48, v195\n"
      "v_mfma_f32_16x16x32_f16 v[72:75], a[100:103], v[100:103], v[72:75]\n"
      "v_pk_mul_f16 v89, v121, v185\n"
      "v_fma_f32 v195, |v65|, v49, v195\n"
      "v_mfma_f32_16x16x32_f16 v[72:75], a[104:107], v[104:107], v[72:75]\n"
      "v_pk_mul_f16 v90, v122, v186\n"
      "v_fma_f32 v195, |v66|, v50, v195\n"
      "v_mfma_f32_16x16x32_f16 v[72:75], a[108:111], v[108:111], v[72:75]\n"
      "v_pk_mul_f16 v91, v123, v187\n"
      "v_fma_f32 v195, |v67|, v51, v195\n"
      "v_mfma_f32_16x16x32_f16 v[76:79], a[124:127], v[108:111], v[28:31]\n"
      "v_pk_mul_f16 v92, v124, v188\n"
      "v_fma_f32 v195, |v68|, v52, v195\n"
      "v_mfma_f32_16x16x32_f16 v[76:79], a[120:123], v[104:107], v[76:79]\n"
      "v_pk_mul_f16 v93, v125, v189\n"
      "v_fma_f32 v195, |v69|, v53, v195\n"
      "v_mfma_f32_16x16x32_f16 v[76:79], a[116:119], v[100:103], v[76:79]\n"
      "v_pk_mul_f16 v94, v126, v190\n"
      "v_fma_f32 v195, |v70|, v54, v195\n"
      "v_mfma_f32_16x16x32_f16 v[76:79], a[112:115], v[96:99], v[76:79]\n"
      "s_waitcnt lgkmcnt(0)\n"
      "v_pk_mul_f16 v95, v127, v191\n"
      "v_fma_f32 v195, |v71|, v55, v195\n"
      "s_cbranch_scc0 .Lhead_s_%=\n"
      "s_branch .Ldispatch_%=\n"
      ".Ltail_%=:\n"
      "v_mfma_f32_16x16x32_f16 v[64:67], a[0:3], v[80:83], v[0:3]\n"
      "v_fma_f32 v195, |v72|, v56, v195\n"
      "v_mfma_f32_16x16x32_f16 v[64:67], a[4:7], v[84:87], v[64:67]\n"
      "v_fma_f32 v195, |v73|, v57, v195\n"
      "v_mfma_f32_16x16x32_f16 v[64:67], a[8:11], v[88:91], v[64:67]\n"
      "v_fma_f32 v195, |v74|, v58, v195\n"
      "v_mfma_f32_16x16x32_f16 v[64:67], a[12:15], v[92:95], v[64:67]\n"
      "v_fma_f32 v195, |v75|, v59, v195\n"
      "v_mfma_f32_16x16x32_f16 v[68:71], a[28:31], v[92:95], v[4:7]\n"
      "v_fma_f32 v195, |v76|, v60, v195\n"
      "v_mfma_f32_16x16x32_f16 v[68:71], a[24:27], v[88:91], v[68:71]\n"
      "v_fma_f32 v195, |v77|, v61, v195\n"
      "v_mfma_f32_16x16x32_f16 v[68:71], a[20:23], v[84:87], v[68:71]\n"
      "v_fma_f32 v195, |v78|, v62, v195\n"
      "v_mfma_f32_16x16x32_f16 v[68:71], a[16:19], v[80:83], v[68:71]\n"
      "v_fma_f32 v195, |v79|, v63, v195\n"
      "v_mfma_f32_16x16x32_f16 v[72:75], a[32:35], v[80:83], v[8:11]\n"
      "v_mul_f32_e64 v192, |v64|, v32\n"
      "v_mfma_f32_16x16x32_f16 v[72:75], a[36:39], v[84:87], v[72:75]\n"
      "v_permlane32_swap_b32 v193, v195\n"
      "v_fma_f32 v192, |v65|, v33, v192\n"
      "v_mfma_f32_16x16x32_f16 v[72:75], a[40:43], v[88:91], v[72:75]\n"
      "v_fma_f32 v192, |v66|, v34, v192\n"
      "v_mfma_f32_16x16x32_f16 v[72:75], a[44:47], v[92:95], v[72:75]\n"
      "v_add_f32 v197, v193, v195\n"
      "v_fma_f32 v192, |v67|, v35, v192\n"
      "v_mfma_f32_16x16x32_f16 v[76:79], a[60:63], v[92:95], v[12:15]\n"
      "v_fma_f32 v192, |v68|, v36, v192\n"
      "v_mfma_f32_16x16x32_f16 v[76:79], a[56:59], v[88:91], v[76:79]\n"
      "v_fma_f32 v192, |v69|, v37, v192\n"
      "v_mfma_f32_16x16x32_f16 v[76:79], a[52:55], v[84:87], v[76:79]\n"
      "v_fma_f32 v192, |v70|, v38, v192\n"
      "v_mfma_f32_16x16x32_f16 v[76:79], a[48:51], v[80:83], v[76:79]\n"
      "v_permlane16_swap_b32 v196, v197\n"
      "v_fma_f32 v192, |v71|, v39, v192\n"
      "v_mfma_f32_16x16x32_f16 v[64:67], a[64:67], v[80:83], v[16:19]\n"
      "v_fma_f32 v192, |v72|, v40, v192\n"
      "v_mfma_f32_16x16x32_f16 v[64:67], a[68:71], v[84:87], v[64:67]\n"
      "v_add_f32 v198, v196, v197\n"
      "v_fma_f32 v192, |v73|, v41, v192\n"
      "v_mfma_f32_16x16x32_f16 v[64:67], a[72:75], v[88:91], v[64:67]\n"
      "v_fma_f32 v192, |v74|, v42, v192\n"
      "v_mfma_f32_16x16x32_f16 v[64:67], a[76:79], v[92:95], v[64:67]\n"
      "ds_write_b32 v199, v198\n"
      "v_fma_f32 v192, |v75|, v43, v192\n"
      "v_mfma_f32_16x16x32_f16 v[68:71], a[92:95], v[92:95], v[20:23]\n"
      "v_fma_f32 v192, |v76|, v44, v192\n"
      "v_mfma_f32_16x16x32_f16 v[68:71], a[88:91], v[88:91], v[68:71]\n"
      "v_fma_f32 v192, |v77|, v45, v192\n"
      "v_mfma_f32_16x16x32_f16 v[68:71], a[84:87], v[84:87], v[68:71]\n"
      "v_fma_f32 v192, |v78|, v46, v192\n"
      "v_mfma_f32_16x16x32_f16 v[68:71], a[80:83], v[80:83], v[68:71]\n"
      "v_fma_f32 v192, |v79|, v47, v192\n"
      "v_mfma_f32_16x16x32_f16 v[72:75], a[96:99], v[80:83], v[24:27]\n"
      "v_fma_f32 v192, |v64|, v48, v192\n"
      "v_mfma_f32_16x16x32_f16 v[72:75], a[100:103], v[84:87], v[72:75]\n"
      "v_fma_f32 v192, |v65|, v49, v192\n"
      "v_mfma_f32_16x16x32_f16 v[72:75], a[104:107], v[88:91], v[72:75]\n"
      "v_fma_f32 v192, |v66|, v50, v192\n"
      "v_mfma_f32_16x16x32_f16 v[72:75], a[108:111], v[92:95], v[72:75]\n"
      "v_fma_f32 v192, |v67|, v51, v192\n"
      "v_mfma_f32_16x16x32_f16 v[76:79], a[124:127], v[92:95], v[28:31]\n"
      "v_fma_f32 v192, |v68|, v52, v192\n"
      "v_mfma_f32_16x16x32_f16 v[76:79], a[120:123], v[88:91], v[76:79]\n"
      "v_fma_f32 v192, |v69|, v53, v192\n"
      "v_mfma_f32_16x16x32_f16 v[76:79], a[116:119], v[84:87], v[76:79]\n"
      "v_fma_f32 v192, |v70|, v54, v192\n"
      "v_mfma_f32_16x16x32_f16 v[76:79], a[112:115], v[80:83], v[76:79]\n"
      "v_fma_f32 v192, |v71|, v55, v192\n"
      "v_add_u32 v199, 2788, v203\n"
      "v_accvgpr_read_b32 v250, a176\n"
      "v_accvgpr_read_b32 v244, a168\n"
      "v_accvgpr_read_b32 v245, a169\n"
      "v_accvgpr_read_b32 v254, a177\n"
      "v_accvgpr_read_b32 v255, a178\n"
      "v_accvgpr_read_b32 v253, a179\n"
      "ds_read_b128 v[16:19], v250 offset:0\n"
      "ds_read_b128 v[20:23], v250 offset:64\n"
      "ds_read_b128 v[24:27], v250 offset:128\n"
      "ds_read_b128 v[28:31], v250 offset:192\n"
      "ds_read_b128 v[112:115], v244 offset:0\n"
      "ds_read_b128 v[116:119], v244 offset:64\n"
      "ds_read_b128 v[120:123], v244 offset:128\n"
      "ds_read_b128 v[124:127], v244 offset:192\n"
      "ds_read_b128 v[128:131], v245 offset:0\n"
      "ds_read_b128 v[132:135], v245 offset:64\n"
      "ds_read_b128 v[136:139], v245 offset:128\n"
      "ds_read_b128 v[140:143], v245 offset:192\n"
      "ds_read_b128 v[0:3], v250 offset:256\n"
      "ds_read_b128 v[4:7], v250 offset:320\n"
      "ds_read_b128 v[8:11], v250 offset:384\n"
      "ds_read_b128 v[12:15], v250 offset:448\n"
      "ds_read_b32 v204, v254\n"
      "v_mov_b32 v224, 0xff7fffff\n"
      "v_mov_b32 v225, 0xff7fffff\n"
      "v_mov_b32 v226, 0xff7fffff\n"
      "v_mov_b32 v227, 0xff7fffff\n"
      "ds_write_b128 v255, v[224:227] offset:2912\n"
      "ds_write_b64 v253, v[224:225] offset:104\n"
      "v_accvgpr_read_b32 v246, a170\n"
      "v_accvgpr_read_b32 v247, a171\n"
      "v_accvgpr_read_b32 v240, a172\n"
      "v_accvgpr_read_b32 v241, a173\n"
      "v_accvgpr_read_b32 v242, a174\n"
      "v_accvgpr_read_b32 v243, a175\n"
      "v_accvgpr_read_b32 v206, a180\n"
      "v_fma_f32 v192, |v72|, v56, v192\n"
      "v_fma_f32 v192, |v73|, v57, v192\n"
      "v_fma_f32 v192, |v74|, v58, v192\n"
      "v_fma_f32 v192, |v75|, v59, v192\n"
      "v_fma_f32 v192, |v76|, v60, v192\n"
      "v_fma_f32 v192, |v77|, v61, v192\n"
      "v_fma_f32 v192, |v78|, v62, v192\n"
      "v_fma_f32 v192, |v79|, v63, v192\n"
      "s_nop 1\n"
      "v_permlane32_swap_b32 v192, v194\n"
      "s_nop 1\n"
      "v_add_f32 v196, v192, v194\n"
      "s_nop 1\n"
      "v_permlane16_swap_b32 v196, v197\n"
      "s_nop 1\n"
      "v_add_f32 v198, v196, v197\n"
      "s_mov_b64 s[98:99], exec\n"
      "s_mov_b64 exec, 0xffff\n"
      "ds_write_b32 v199, v198\n"
      "s_mov_b64 exec, s[98:99]\n"
      "s_branch .Lgram_%=\n"
      ".Ldrain_%=:\n"
      "v_accvgpr_read_b32 v250, a176\n"
      "v_accvgpr_read_b32 v244, a168\n"
      "v_accvgpr_read_b32 v245, a169\n"
      "v_accvgpr_read_b32 v254, a177\n"
      "v_accvgpr_read_b32 v255, a178\n"
      "v_accvgpr_read_b32 v253, a179\n"
      "ds_read_b128 v[16:19], v250 offset:0\n"
      "ds_read_b128 v[20:23], v250 offset:64\n"
      "ds_read_b128 v[24:27], v250 offset:128\n"
      "ds_read_b128 v[28:31], v250 offset:192\n"
      "ds_read_b128 v[112:115], v244 offset:0\n"
      "ds_read_b128 v[116:119], v244 offset:64\n"
      "ds_read_b128 v[120:123], v244 offset:128\n"
      "ds_read_b128 v[124:127], v244 offset:192\n"
      "ds_read_b128 v[128:131], v245 offset:0\n"
      "ds_read_b128 v[132:135], v245 offset:64\n"
      "ds_read_b128 v[136:139], v245 offset:128\n"
      "ds_read_b128 v[140:143], v245 offset:192\n"
      "ds_read_b128 v[0:3], v250 offset:256\n"
      "ds_read_b128 v[4:7], v250 offset:320\n"
      "ds_read_b128 v[8:11], v250 offset:384\n"
      "ds_read_b128 v[12:15], v250 offset:448\n"
      "ds_read_b32 v204, v254\n"
      "v_mov_b32 v224, 0xff7fffff\n"
      "v_mov_b32 v225, 0xff7fffff\n"
      "v_mov_b32 v226, 0xff7fffff\n"
      "v_mov_b32 v227, 0xff7fffff\n"
      "ds_write_b128 v255, v[224:227] offset:2912\n"
      "ds_write_b64 v253, v[224:225] offset:104\n"
      "v_accvgpr_read_b32 v246, a170\n"
      "v_accvgpr_read_b32 v247, a171\n"
      "v_accvgpr_read_b32 v240, a172\n"
      "v_accvgpr_read_b32 v241, a173\n"
      "v_accvgpr_read_b32 v242, a174\n"
      "v_accvgpr_read_b32 v243, a175\n"
      "v_accvgpr_read_b32 v206, a180\n"
      "v_fma_f32 v195, |v72|, v56, v195\n"
      "v_fma_f32 v195, |v73|, v57, v195\n"
      "v_fma_f32 v195, |v74|, v58, v195\n"
      "v_fma_f32 v195, |v75|, v59, v195\n"
      "v_fma_f32 v195, |v76|, v60, v195\n"
      "v_fma_f32 v195, |v77|, v61, v195\n"
      "v_fma_f32 v195, |v78|, v62, v195\n"
      "v_fma_f32 v195, |v79|, v63, v195\n"
      "s_nop 1\n"
      "v_permlane32_swap_b32 v193, v195\n"
      "s_nop 1\n"
      "v_add_f32 v197, v193, v195\n"
      "s_nop 1\n"
      "v_permlane16_swap_b32 v196, v197\n"
      "s_nop 1\n"
      "v_add_f32 v198, v196, v197\n"
      "ds_write_b32 v199, v198\n"
      ".Lgram_%=:\n"
      "v_lshl_add_u32 v201, v249, 2, 0\n"
      "v_cmp_gt_u32 s[70:71], v201, v248\n"
      "v_lshl_add_u32 v201, v249, 2, 1\n"
      "v_cmp_gt_u32 s[72:73], v201, v248\n"
      "v_lshl_add_u32 v201, v249, 2, 2\n"
      "v_cmp_gt_u32 s[74:75], v201, v248\n"
      "v_lshl_add_u32 v201, v249, 2, 3\n"
      "v_cmp_gt_u32 s[76:77], v201, v248\n"
      "s_waitcnt lgkmcnt(0)\n"
      "s_barrier\n"
      "ds_read_b128 v[144:147], v246 offset:0\n"
      "ds_read_b128 v[148:151], v247 offset:0\n"
      "ds_read_b32 v152, v240 offset:0\n"
      "ds_read_b32 v153, v241 offset:0\n"
      "ds_read_b32 v154, v242 offset:0\n"
      "ds_read_b32 v155, v243 offset:0\n"
      "v_lshrrev_b32 v204, 23, v204\n"
      "v_lshlrev_b32 v204, 1, v204\n"
      "v_sub_u32 v204, 381, v204\n"
      "v_lshlrev_b32 v204, 23, v204\n"
      "v_mul_f32 v205, s67, v204\n"
      "v_mul_f32 v204, s66, v204\n"
      "v_mul_f32 v204, 0x3f38aa3b, v204\n"
      "v_pk_mul_f16 v208, v112, v16\n"
      "v_pk_mul_f16 v209, v113, v17\n"
      "v_pk_mul_f16 v210, v114, v18\n"
      "v_pk_mul_f16 v211, v115, v19\n"
      "v_pk_mul_f16 v212, v116, v20\n"
      "v_pk_mul_f16 v213, v117, v21\n"
      "v_pk_mul_f16 v214, v118, v22\n"
      "v_pk_mul_f16 v215, v119, v23\n"
      "v_pk_mul_f16 v216, v120, v24\n"
      "v_pk_mul_f16 v217, v121, v25\n"
      "v_pk_mul_f16 v218, v122, v26\n"
      "v_pk_mul_f16 v219, v123, v27\n"
      "v_pk_mul_f16 v220, v124, v28\n"
      "v_pk_mul_f16 v221, v125, v29\n"
      "v_pk_mul_f16 v222, v126, v30\n"
      "v_pk_mul_f16 v223, v127, v31\n"
      "s_waitcnt lgkmcnt(0)\n"
      "v_mfma_f32_16x16x32_f16 v[144:147], v[112:115], v[208:211], v[144:147]\n"
      "v_pk_mul_f16 v224, v112, v0\n"
      "v_pk_mul_f16 v225, v113, v1\n"
      "ds_read_b128 v[80:83], v244 offset:7072\n"
      "v_mfma_f32_16x16x32_f16 v[144:147], v[116:119], v[212:215], v[144:147]\n"
      "v_pk_mul_f16 v226, v114, v2\n"
      "v_pk_mul_f16 v227, v115, v3\n"
      "ds_read_b128 v[84:87], v244 offset:7136\n"
      "v_mfma_f32_16x16x32_f16 v[144:147], v[120:123], v[216:219], v[144:147]\n"
      "v_pk_mul_f16 v228, v116, v4\n"
      "v_pk_mul_f16 v229, v117, v5\n"
      "ds_read_b128 v[88:91], v244 offset:7200\n"
      "v_mfma_f32_16x16x32_f16 v[144:147], v[124:127], v[220:223], v[144:147]\n"
      "v_pk_mul_f16 v230, v118, v6\n"
      "v_pk_mul_f16 v231, v119, v7\n"
      "ds_read_b128 v[92:95], v244 offset:7264\n"
      "v_mfma_f32_16x16x32_f16 v[148:151], v[128:131], v[208:211], v[148:151]\n"
      "v_pk_mul_f16 v232, v120, v8\n"
      "v_pk_mul_f16 v233, v121, v9\n"
      "ds_read_b128 v[96:99], v245 offset:7072\n"
      "v_mfma_f32_16x16x32_f16 v[148:151], v[132:135], v[212:215], v[148:151]\n"
      "v_pk_mul_f16 v234, v122, v10\n"
      "v_pk_mul_f16 v235, v123, v11\n"
      "ds_read_b128 v[100:103], v245 offset:7136\n"
      "v_mfma_f32_16x16x32_f16 v[148:151], v[136:139], v[216:219], v[148:151]\n"
      "v_pk_mul_f16 v236, v124, v12\n"
      "v_pk_mul_f16 v237, v125, v13\n"
      "ds_read_b128 v[104:107], v245 offset:7200\n"
      "v_mfma_f32_16x16x32_f16 v[148:151], v[140:143], v[220:223], v[148:151]\n"
      "v_pk_mul_f16 v238, v126, v14\n"
      "v_pk_mul_f16 v239, v127, v15\n"
      "ds_read_b128 v[108:111], v245 offset:7264\n"
      "v_mfma_f32_16x16x32_f16 v[32:35], v[112:115], v[224:227], 0\n"
      "v_pk_mul_f16 v208, v128, v16\n"
      "v_pk_mul_f16 v209, v129, v17\n"
      "v_mfma_f32_16x16x32_f16 v[32:35], v[116:119], v[228:231], v[32:35]\n"
      "v_pk_mul_f16 v210, v130, v18\n"
      "ds_read_b128 v[156:159], v246 offset:2928\n"
      "v_pk_mul_f16 v211, v131, v19\n"
      "v_mfma_f32_16x16x32_f16 v[32:35], v[120:123], v[232:235], v[32:35]\n"
      "v_pk_mul_f16 v212, v132, v20\n"
      "v_pk_mul_f16 v213, v133, v21\n"
      "ds_read_b128 v[160:163], v247 offset:2928\n"
      "v_mfma_f32_16x16x32_f16 v[32:35], v[124:127], v[236:239], v[32:35]\n"
      "v_pk_mul_f16 v214, v134, v22\n"
      "v_pk_mul_f16 v215, v135, v23\n"
      "ds_read_b32 v164, v240 offset:2928\n"
      "v_mfma_f32_16x16x32_f16 v[36:39], v[128:131], v[224:227], 0\n"
      "v_pk_mul_f16 v216, v136, v24\n"
      "v_pk_mul_f16 v217, v137, v25\n"
      "v_mfma_f32_16x16x32_f16 v[36:39], v[132:135], v[228:231], v[36:39]\n"
      "v_pk_mul_f16 v218, v138, v26\n"
      "ds_read_b32 v165, v241 offset:2928\n"
      "v_pk_mul_f16 v219, v139, v27\n"
      "v_mfma_f32_16x16x32_f16 v[36:39], v[136:139], v[232:235], v[36:39]\n"
      "v_pk_mul_f16 v220, v140, v28\n"
      "v_pk_mul_f16 v221, v141, v29\n"
      "ds_read_b32 v166, v242 offset:2928\n"
      "v_mfma_f32_16x16x32_f16 v[36:39], v[140:143], v[236:239], v[36:39]\n"
      "v_pk_mul_f16 v222, v142, v30\n"
      "v_pk_mul_f16 v223, v143, v31\n"
      "ds_read_b32 v167, v243 offset:2928\n"
      "v_mfma_f32_16x16x32_f16 v[152:155], v[128:131], v[208:211], v[152:155]\n"
      "v_pk_mul_f16 v224, v128, v0\n"
      "v_pk_mul_f16 v225, v129, v1\n"
      "v_pk_mul_f16 v226, v130, v2\n"
      "v_pk_mul_f16 v227, v131, v3\n"
      "v_mfma_f32_16x16x32_f16 v[152:155], v[132:135], v[212:215], v[152:155]\n"
      "v_pk_mul_f16 v228, v132, v4\n"
      "v_pk_mul_f16 v229, v133, v5\n"
      "v_pk_mul_f16 v230, v134, v6\n"
      "v_pk_mul_f16 v231, v135, v7\n"
      "v_mfma_f32_16x16x32_f16 v[152:155], v[136:139], v[216:219], v[152:155]\n"
      "v_pk_mul_f16 v232, v136, v8\n"
      "v_pk_mul_f16 v233, v137, v9\n"
      "v_pk_mul_f16 v234, v138, v10\n"
      "v_pk_mul_f16 v235, v139, v11\n"
      "v_mfma_f32_16x16x32_f16 v[152:155], v[140:143], v[220:223], v[152:155]\n"
      "v_pk_mul_f16 v236, v140, v12\n"
      "v_pk_mul_f16 v237, v141, v13\n"
      "v_pk_mul_f16 v238, v142, v14\n"
      "v_pk_mul_f16 v239, v143, v15\n"
      "s_waitcnt lgkmcnt(6)\n"
      "v_mfma_f32_16x16x32_f16 v[40:43], v[128:131], v[224:227], 0\n"
      "v_pk_mul_f16 v208, v80, v16\n"
      "v_pk_mul_f16 v209, v81, v17\n"
      "v_pk_mul_f16 v210, v82, v18\n"
      "v_pk_mul_f16 v211, v83, v19\n"
      "v_mfma_f32_16x16x32_f16 v[40:43], v[132:135], v[228:231], v[40:43]\n"
      "v_pk_mul_f16 v212, v84, v20\n"
      "v_pk_mul_f16 v213, v85, v21\n"
      "v_pk_mul_f16 v214, v86, v22\n"
      "v_pk_mul_f16 v215, v87, v23\n"
      "v_mfma_f32_16x16x32_f16 v[40:43], v[136:139], v[232:235], v[40:43]\n"
      "v_pk_mul_f16 v216, v88, v24\n"
      "v_pk_mul_f16 v217, v89, v25\n"
      "v_pk_mul_f16 v218, v90, v26\n"
      "v_pk_mul_f16 v219, v91, v27\n"
      "v_mfma_f32_16x16x32_f16 v[40:43], v[140:143], v[236:239], v[40:43]\n"
      "v_pk_mul_f16 v220, v92, v28\n"
      "v_pk_mul_f16 v221, v93, v29\n"
      "v_pk_mul_f16 v222, v94, v30\n"
      "v_pk_mul_f16 v223, v95, v31\n"
      "s_waitcnt lgkmcnt(0)\n"
      "v_mfma_f32_16x16x32_f16 v[156:159], v[80:83], v[208:211], v[156:159]\n"
      "v_pk_mul_f16 v224, v80, v0\n"
      "v_pk_mul_f16 v225, v81, v1\n"
      "ds_read_b128 v[112:115], v244 offset:14144\n"
      "v_mfma_f32_16x16x32_f16 v[156:159], v[84:87], v[212:215], v[156:159]\n"
      "v_pk_mul_f16 v226, v82, v2\n"
      "v_pk_mul_f16 v227, v83, v3\n"
      "ds_read_b128 v[116:119], v244 offset:14208\n"
      "v_mfma_f32_16x16x32_f16 v[156:159], v[88:91], v[216:219], v[156:159]\n"
      "v_pk_mul_f16 v228, v84, v4\n"
      "v_pk_mul_f16 v229, v85, v5\n"
      "ds_read_b128 v[120:123], v244 offset:14272\n"
      "v_mfma_f32_16x16x32_f16 v[156:159], v[92:95], v[220:223], v[156:159]\n"
      "v_pk_mul_f16 v230, v86, v6\n"
      "v_pk_mul_f16 v231, v87, v7\n"
      "ds_read_b128 v[124:127], v244 offset:14336\n"
      "v_mfma_f32_16x16x32_f16 v[160:163], v[96:99], v[208:211], v[160:163]\n"
      "v_pk_mul_f16 v232, v88, v8\n"
      "v_pk_mul_f16 v233, v89, v9\n"
      "ds_read_b128 v[128:131], v245 offset:14144\n"
      "v_mfma_f32_16x16x32_f16 v[160:163], v[100:103], v[212:215], v[160:163]\n"
      "v_pk_mul_f16 v234, v90, v10\n"
      "v_pk_mul_f16 v235, v91, v11\n"
      "ds_read_b128 v[132:135], v245 offset:14208\n"
      "v_mfma_f32_16x16x32_f16 v[160:163], v[104:107], v[216:219], v[160:163]\n"
      "v_pk_mul_f16 v236, v92, v12\n"
      "v_pk_mul_f16 v237, v93, v13\n"
      "ds_read_b128 v[136:139], v245 offset:14272\n"
      "v_mfma_f32_16x16x32_f16 v[160:163], v[108:111], v[220:223], v[160:163]\n"
      "v_pk_mul_f16 v238, v94, v14\n"
      "v_pk_mul_f16 v239, v95, v15\n"
      "ds_read_b128 v[140:143], v245 offset:14336\n"
      "v_mfma_f32_16x16x32_f16 v[44:47], v[80:83], v[224:227], 0\n"
      "v_pk_mul_f16 v208, v96, v16\n"
      "v_pk_mul_f16 v209, v97, v17\n"
      "v_mfma_f32_16x16x32_f16 v[44:47], v[84:87], v[228:231], v[44:47]\n"
      "v_pk_mul_f16 v210, v98, v18\n"
      "ds_read_b128 v[168:171], v246 offset:5856\n"
      "v_pk_mul_f16 v211, v99, v19\n"
      "v_mfma_f32_16x16x32_f16 v[44:47], v[88:91], v[232:235], v[44:47]\n"
      "v_pk_mul_f16 v212, v100, v20\n"
      "v_pk_mul_f16 v213, v101, v21\n"
      "ds_read_b128 v[172:175], v247 offset:5856\n"
      "v_mfma_f32_16x16x32_f16 v[44:47], v[92:95], v[236:239], v[44:47]\n"
      "v_pk_mul_f16 v214, v102, v22\n"
      "v_pk_mul_f16 v215, v103, v23\n"
      "ds_read_b32 v176, v240 offset:5856\n"
      "v_mfma_f32_16x16x32_f16 v[48:51], v[96:99], v[224:227], 0\n"
      "v_pk_mul_f16 v216, v104, v24\n"
      "v_pk_mul_f16 v217, v105, v25\n"
      "v_mfma_f32_16x16x32_f16 v[48:51], v[100:103], v[228:231], v[48:51]\n"
      "v_pk_mul_f16 v218, v106, v26\n"
      "ds_read_b32 v177, v241 offset:5856\n"
      "v_pk_mul_f16 v219, v107, v27\n"
      "v_mfma_f32_16x16x32_f16 v[48:51], v[104:107], v[232:235], v[48:51]\n"
      "v_pk_mul_f16 v220, v108, v28\n"
      "v_pk_mul_f16 v221, v109, v29\n"
      "ds_read_b32 v178, v242 offset:5856\n"
      "v_mfma_f32_16x16x32_f16 v[48:51], v[108:111], v[236:239], v[48:51]\n"
      "v_pk_mul_f16 v222, v110, v30\n"
      "v_pk_mul_f16 v223, v111, v31\n"
      "ds_read_b32 v179, v243 offset:5856\n"
      "v_mfma_f32_16x16x32_f16 v[164:167], v[96:99], v[208:211], v[164:167]\n"
      "v_pk_mul_f16 v224, v96, v0\n"
      "v_pk_mul_f16 v225, v97, v1\n"
      "v_pk_mul_f16 v226, v98, v2\n"
      "v_pk_mul_f16 v227, v99, v3\n"
      "v_mfma_f32_16x16x32_f16 v[164:167], v[100:103], v[212:215], v[164:167]\n"
      "v_pk_mul_f16 v228, v100, v4\n"
      "v_pk_mul_f16 v229, v101, v5\n"
      "v_pk_mul_f16 v230, v102, v6\n"
      "v_pk_mul_f16 v231, v103, v7\n"
      "v_mfma_f32_16x16x32_f16 v[164:167], v[104:107], v[216:219], v[164:167]\n"
      "v_pk_mul_f16 v232, v104, v8\n"
      "v_pk_mul_f16 v233, v105, v9\n"
      "v_pk_mul_f16 v234, v106, v10\n"
      "v_pk_mul_f16 v235, v107, v11\n"
      "v_mfma_f32_16x16x32_f16 v[164:167], v[108:111], v[220:223], v[164:167]\n"
      "v_pk_mul_f16 v236, v108, v12\n"
      "v_pk_mul_f16 v237, v109, v13\n"
      "v_pk_mul_f16 v238, v110, v14\n"
      "v_pk_mul_f16 v239, v111, v15\n"
      "s_waitcnt lgkmcnt(6)\n"
      "v_mfma_f32_16x16x32_f16 v[52:55], v[96:99], v[224:227], 0\n"
      "v_pk_mul_f16 v208, v112, v16\n"
      "v_pk_mul_f16 v209, v113, v17\n"
      "v_pk_mul_f16 v210, v114, v18\n"
      "v_pk_mul_f16 v211, v115, v19\n"
      "v_mfma_f32_16x16x32_f16 v[52:55], v[100:103], v[228:231], v[52:55]\n"
      "v_pk_mul_f16 v212, v116, v20\n"
      "v_pk_mul_f16 v213, v117, v21\n"
      "v_pk_mul_f16 v214, v118, v22\n"
      "v_pk_mul_f16 v215, v119, v23\n"
      "v_mfma_f32_16x16x32_f16 v[52:55], v[104:107], v[232:235], v[52:55]\n"
      "v_pk_mul_f16 v216, v120, v24\n"
      "v_pk_mul_f16 v217, v121, v25\n"
      "v_pk_mul_f16 v218, v122, v26\n"
      "v_pk_mul_f16 v219, v123, v27\n"
      "v_mfma_f32_16x16x32_f16 v[52:55], v[108:111], v[236:239], v[52:55]\n"
      "v_pk_mul_f16 v220, v124, v28\n"
      "v_pk_mul_f16 v221, v125, v29\n"
      "v_pk_mul_f16 v222, v126, v30\n"
      "v_pk_mul_f16 v223, v127, v31\n"
      "s_waitcnt lgkmcnt(0)\n"
      "v_mfma_f32_16x16x32_f16 v[168:171], v[112:115], v[208:211], v[168:171]\n"
      "v_pk_mul_f16 v224, v112, v0\n"
      "v_pk_mul_f16 v225, v113, v1\n"
      "ds_read_b128 v[80:83], v244 offset:21216\n"
      "v_mfma_f32_16x16x32_f16 v[168:171], v[116:119], v[212:215], v[168:171]\n"
      "v_pk_mul_f16 v226, v114, v2\n"
      "v_pk_mul_f16 v227, v115, v3\n"
      "ds_read_b128 v[84:87], v244 offset:21280\n"
      "v_mfma_f32_16x16x32_f16 v[168:171], v[120:123], v[216:219], v[168:171]\n"
      "v_pk_mul_f16 v228, v116, v4\n"
      "v_pk_mul_f16 v229, v117, v5\n"
      "ds_read_b128 v[88:91], v244 offset:21344\n"
      "v_mfma_f32_16x16x32_f16 v[168:171], v[124:127], v[220:223], v[168:171]\n"
      "v_pk_mul_f16 v230, v118, v6\n"
      "v_pk_mul_f16 v231, v119, v7\n"
      "ds_read_b128 v[92:95], v244 offset:21408\n"
      "v_mfma_f32_16x16x32_f16 v[172:175], v[128:131], v[208:211], v[172:175]\n"
      "v_pk_mul_f16 v232, v120, v8\n"
      "v_pk_mul_f16 v233, v121, v9\n"
      "ds_read_b128 v[96:99], v245 offset:21216\n"
      "v_mfma_f32_16x16x32_f16 v[172:175], v[132:135], v[212:215], v[172:175]\n"
      "v_pk_mul_f16 v234, v122, v10\n"
      "v_pk_mul_f16 v235, v123, v11\n"
      "ds_read_b128 v[100:103], v245 offset:21280\n"
      "v_mfma_f32_16x16x32_f16 v[172:175], v[136:139], v[216:219], v[172:175]\n"
      "v_pk_mul_f16 v236, v124, v12\n"
      "v_pk_mul_f16 v237, v125, v13\n"
      "ds_read_b128 v[104:107], v245 offset:21344\n"
      "v_mfma_f32_16x16x32_f16 v[172:175], v[140:143], v[220:223], v[172:175]\n"
      "v_pk_mul_f16 v238, v126, v14\n"
      "v_pk_mul_f16 v239, v127, v15\n"
      "ds_read_b128 v[108:111], v245 offset:21408\n"
      "v_mfma_f32_16x16x32_f16 v[56:59], v[112:115], v[224:227], 0\n"
      "v_pk_mul_f16 v208, v128, v16\n"
      "v_pk_mul_f16 v209, v129, v17\n"
      "v_mfma_f32_16x16x32_f16 v[56:59], v[116:119], v[228:231], v[56:59]\n"
      "v_pk_mul_f16 v210, v130, v18\n"
      "ds_read_b128 v[180:183], v246 offset:8784\n"
      "v_pk_mul_f16 v211, v131, v19\n"
      "v_mfma_f32_16x16x32_f16 v[56:59], v[120:123], v[232:235], v[56:59]\n"
      "v_pk_mul_f16 v212, v132, v20\n"
      "v_pk_mul_f16 v213, v133, v21\n"
      "ds_read_b128 v[184:187], v247 offset:8784\n"
      "v_mfma_f32_16x16x32_f16 v[56:59], v[124:127], v[236:239], v[56:59]\n"
      "v_pk_mul_f16 v214, v134, v22\n"
      "v_pk_mul_f16 v215, v135, v23\n"
      "ds_read_b32 v188, v240 offset:8784\n"
      "v_mfma_f32_16x16x32_f16 v[60:63], v[128:131], v[224:227], 0\n"
      "v_pk_mul_f16 v216, v136, v24\n"
      "v_pk_mul_f16 v217, v137, v25\n"
      "v_mfma_f32_16x16x32_f16 v[60:63], v[132:135], v[228:231], v[60:63]\n"
      "v_pk_mul_f16 v218, v138, v26\n"
      "ds_read_b32 v189, v241 offset:8784\n"
      "v_pk_mul_f16 v219, v139, v27\n"
      "v_mfma_f32_16x16x32_f16 v[60:63], v[136:139], v[232:235], v[60:63]\n"
      "v_pk_mul_f16 v220, v140, v28\n"
      "v_pk_mul_f16 v221, v141, v29\n"
      "ds_read_b32 v190, v242 offset:8784\n"
      "v_mfma_f32_16x16x32_f16 v[60:63], v[140:143], v[236:239], v[60:63]\n"
      "v_pk_mul_f16 v222, v142, v30\n"
      "v_pk_mul_f16 v223, v143, v31\n"
      "ds_read_b32 v191, v243 offset:8784\n"
      "v_mfma_f32_16x16x32_f16 v[176:179], v[128:131], v[208:211], v[176:179]\n"
      "v_pk_mul_f16 v224, v128, v0\n"
      "v_pk_mul_f16 v225, v129, v1\n"
      "v_pk_mul_f16 v226, v130, v2\n"
      "v_pk_mul_f16 v227, v131, v3\n"
      "v_mfma_f32_16x16x32_f16 v[176:179], v[132:135], v[212:215], v[176:179]\n"
      "v_pk_mul_f16 v228, v132, v4\n"
      "v_pk_mul_f16 v229, v133, v5\n"
      "v_pk_mul_f16 v230, v134, v6\n"
      "v_pk_mul_f16 v231, v135, v7\n"
      "v_mfma_f32_16x16x32_f16 v[176:179], v[136:139], v[216:219], v[176:179]\n"
      "v_pk_mul_f16 v232, v136, v8\n"
      "v_pk_mul_f16 v233, v137, v9\n"
      "v_pk_mul_f16 v234, v138, v10\n"
      "v_pk_mul_f16 v235, v139, v11\n"
      "v_mfma_f32_16x16x32_f16 v[176:179], v[140:143], v[220:223], v[176:179]\n"
      "v_pk_mul_f16 v236, v140, v12\n"
      "v_pk_mul_f16 v237, v141, v13\n"
      "v_pk_mul_f16 v238, v142, v14\n"
      "v_pk_mul_f16 v239, v143, v15\n"
      "s_waitcnt lgkmcnt(6)\n"
      "v_mfma_f32_16x16x32_f16 v[64:67], v[128:131], v[224:227], 0\n"
      "v_pk_mul_f16 v208, v80, v16\n"
      "v_pk_mul_f16 v209, v81, v17\n"
      "v_pk_mul_f16 v210, v82, v18\n"
      "v_pk_mul_f16 v211, v83, v19\n"
      "v_mfma_f32_16x16x32_f16 v[64:67], v[132:135], v[228:231], v[64:67]\n"
      "v_pk_mul_f16 v212, v84, v20\n"
      "v_pk_mul_f16 v213, v85, v21\n"
      "v_pk_mul_f16 v214, v86, v22\n"
      "v_pk_mul_f16 v215, v87, v23\n"
      "v_mfma_f32_16x16x32_f16 v[64:67], v[136:139], v[232:235], v[64:67]\n"
      "v_pk_mul_f16 v216, v88, v24\n"
      "v_pk_mul_f16 v217, v89, v25\n"
      "v_pk_mul_f16 v218, v90, v26\n"
      "v_pk_mul_f16 v219, v91, v27\n"
      "v_mfma_f32_16x16x32_f16 v[64:67], v[140:143], v[236:239], v[64:67]\n"
      "v_pk_mul_f16 v220, v92, v28\n"
      "v_pk_mul_f16 v221, v93, v29\n"
      "v_pk_mul_f16 v222, v94, v30\n"
      "v_pk_mul_f16 v223, v95, v31\n"
      "s_waitcnt lgkmcnt(0)\n"
      "v_mfma_f32_16x16x32_f16 v[180:183], v[80:83], v[208:211], v[180:183]\n"
      "v_pk_mul_f16 v224, v80, v0\n"
      "v_pk_mul_f16 v225, v81, v1\n"
      "v_mfma_f32_16x16x32_f16 v[180:183], v[84:87], v[212:215], v[180:183]\n"
      "v_pk_mul_f16 v226, v82, v2\n"
      "v_pk_mul_f16 v227, v83, v3\n"
      "v_mfma_f32_16x16x32_f16 v[180:183], v[88:91], v[216:219], v[180:183]\n"
      "v_pk_mul_f16 v228, v84, v4\n"
      "v_pk_mul_f16 v229, v85, v5\n"
      "v_mfma_f32_16x16x32_f16 v[180:183], v[92:95], v[220:223], v[180:183]\n"
      "v_pk_mul_f16 v230, v86, v6\n"
      "v_pk_mul_f16 v231, v87, v7\n"
      "v_mfma_f32_16x16x32_f16 v[184:187], v[96:99], v[208:211], v[184:187]\n"
      "v_pk_mul_f16 v232, v88, v8\n"
      "v_pk_mul_f16 v233, v89, v9\n"
      "v_mfma_f32_16x16x32_f16 v[184:187], v[100:103], v[212:215], v[184:187]\n"
      "v_pk_mul_f16 v234, v90, v10\n"
      "v_pk_mul_f16 v235, v91, v11\n"
      "v_mfma_f32_16x16x32_f16 v[184:187], v[104:107], v[216:219], v[184:187]\n"
      "v_pk_mul_f16 v236, v92, v12\n"
      "v_pk_mul_f16 v237, v93, v13\n"
      "v_mfma_f32_16x16x32_f16 v[184:187], v[108:111], v[220:223], v[184:187]\n"
      "v_pk_mul_f16 v238, v94, v14\n"
      "v_pk_mul_f16 v239, v95, v15\n"
      "v_mfma_f32_16x16x32_f16 v[68:71], v[80:83], v[224:227], 0\n"
      "v_pk_mul_f16 v208, v96, v16\n"
      "v_pk_mul_f16 v209, v97, v17\n"
      "v_mfma_f32_16x16x32_f16 v[68:71], v[84:87], v[228:231], v[68:71]\n"
      "v_pk_mul_f16 v210, v98, v18\n"
      "v_pk_mul_f16 v211, v99, v19\n"
      "v_mfma_f32_16x16x32_f16 v[68:71], v[88:91], v[232:235], v[68:71]\n"
      "v_pk_mul_f16 v212, v100, v20\n"
      "v_pk_mul_f16 v213, v101, v21\n"
      "v_mfma_f32_16x16x32_f16 v[68:71], v[92:95], v[236:239], v[68:71]\n"
      "v_pk_mul_f16 v214, v102, v22\n"
      "v_pk_mul_f16 v215, v103, v23\n"
      "v_mfma_f32_16x16x32_f16 v[72:75], v[96:99], v[224:227], 0\n"
      "v_pk_mul_f16 v216, v104, v24\n"
      "v_pk_mul_f16 v217, v105, v25\n"
      "v_mfma_f32_16x16x32_f16 v[72:75], v[100:103], v[228:231], v[72:75]\n"
      "v_pk_mul_f16 v218, v106, v26\n"
      "v_pk_mul_f16 v219, v107, v27\n"
      "v_mfma_f32_16x16x32_f16 v[72:75], v[104:107], v[232:235], v[72:75]\n"
      "v_pk_mul_f16 v220, v108, v28\n"
      "v_pk_mul_f16 v221, v109, v29\n"
      "v_mfma_f32_16x16x32_f16 v[72:75], v[108:111], v[236:239], v[72:75]\n"
      "v_pk_mul_f16 v222, v110, v30\n"
      "v_pk_mul_f16 v223, v111, v31\n"
      "v_mfma_f32_16x16x32_f16 v[188:191], v[96:99], v[208:211], v[188:191]\n"
      "v_pk_mul_f16 v224, v96, v0\n"
      "v_pk_mul_f16 v225, v97, v1\n"
      "v_pk_mul_f16 v226, v98, v2\n"
      "v_pk_mul_f16 v227, v99, v3\n"
      "v_mfma_f32_16x16x32_f16 v[188:191], v[100:103], v[212:215], v[188:191]\n"
      "v_pk_mul_f16 v228, v100, v4\n"
      "v_pk_mul_f16 v229, v101, v5\n"
      "v_pk_mul_f16 v230, v102, v6\n"
      "v_pk_mul_f16 v231, v103, v7\n"
      "v_mfma_f32_16x16x32_f16 v[188:191], v[104:107], v[216:219], v[188:191]\n"
      "v_pk_mul_f16 v232, v104, v8\n"
      "v_pk_mul_f16 v233, v105, v9\n"
      "v_pk_mul_f16 v234, v106, v10\n"
      "v_pk_mul_f16 v235, v107, v11\n"
      "v_mfma_f32_16x16x32_f16 v[188:191], v[108:111], v[220:223], v[188:191]\n"
      "v_pk_mul_f16 v236, v108, v12\n"
      "v_pk_mul_f16 v237, v109, v13\n"
      "v_pk_mul_f16 v238, v110, v14\n"
      "v_pk_mul_f16 v239, v111, v15\n"
      "v_mfma_f32_16x16x32_f16 v[76:79], v[96:99], v[224:227], 0\n"
      "v_mfma_f32_16x16x32_f16 v[76:79], v[100:103], v[228:231], v[76:79]\n"
      "v_mfma_f32_16x16x32_f16 v[76:79], v[104:107], v[232:235], v[76:79]\n"
      "v_mfma_f32_16x16x32_f16 v[76:79], v[108:111], v[236:239], v[76:79]\n"
      "v_cndmask_b32 v144, v152, v144, s[70:71]\n"
      "v_cndmask_b32 v145, v153, v145, s[72:73]\n"
      "v_cndmask_b32 v146, v154, v146, s[74:75]\n"
      "v_cndmask_b32 v147, v155, v147, s[76:77]\n"
      "v_cndmask_b32 v32, v40, v32, s[70:71]\n"
      "v_cndmask_b32 v33, v41, v33, s[72:73]\n"
      "v_cndmask_b32 v34, v42, v34, s[74:75]\n"
      "v_cndmask_b32 v35, v43, v35, s[76:77]\n"
      "v_cndmask_b32 v156, v164, v156, s[70:71]\n"
      "v_cndmask_b32 v157, v165, v157, s[72:73]\n"
      "v_cndmask_b32 v158, v166, v158, s[74:75]\n"
      "v_cndmask_b32 v159, v167, v159, s[76:77]\n"
      "v_cndmask_b32 v44, v52, v44, s[70:71]\n"
      "v_cndmask_b32 v45, v53, v45, s[72:73]\n"
      "v_cndmask_b32 v46, v54, v46, s[74:75]\n"
      "v_cndmask_b32 v47, v55, v47, s[76:77]\n"
      "v_cndmask_b32 v168, v176, v168, s[70:71]\n"
      "v_cndmask_b32 v169, v177, v169, s[72:73]\n"
      "v_cndmask_b32 v170, v178, v170, s[74:75]\n"
      "v_cndmask_b32 v171, v179, v171, s[76:77]\n"
      "v_cndmask_b32 v56, v64, v56, s[70:71]\n"
      "v_cndmask_b32 v57, v65, v57, s[72:73]\n"
      "v_cndmask_b32 v58, v66, v58, s[74:75]\n"
      "v_cndmask_b32 v59, v67, v59, s[76:77]\n"
      "v_cndmask_b32 v180, v188, v180, s[70:71]\n"
      "v_cndmask_b32 v181, v189, v181, s[72:73]\n"
      "v_cndmask_b32 v182, v190, v182, s[74:75]\n"
      "v_cndmask_b32 v183, v191, v183, s[76:77]\n"
      "v_cndmask_b32 v68, v76, v68, s[70:71]\n"
      "v_cndmask_b32 v69, v77, v69, s[72:73]\n"
      "v_cndmask_b32 v70, v78, v70, s[74:75]\n"
      "v_cndmask_b32 v71, v79, v71, s[76:77]\n"
      "v_max3_f32 v192, v144, v145, v146\n"
      "v_min3_f32 v152, v144, v145, v146\n"
      "v_max3_f32 v192, v147, v148, v192\n"
      "v_min3_f32 v152, v147, v148, v152\n"
      "v_max3_f32 v192, v149, v150, v192\n"
      "v_min3_f32 v152, v149, v150, v152\n"
      "v_max_f32 v192, v151, v192\n"
      "v_min_f32 v152, v151, v152\n"
      "v_max3_f32 v193, v156, v157, v158\n"
      "v_min3_f32 v164, v156, v157, v158\n"
      "v_max3_f32 v193, v159, v160, v193\n"
      "v_min3_f32 v164, v159, v160, v164\n"
      "v_max3_f32 v193, v161, v162, v193\n"
      "v_min3_f32 v164, v161, v162, v164\n"
      "v_max_f32 v193, v163, v193\n"
      "v_min_f32 v164, v163, v164\n"
      "v_max3_f32 v194, v168, v169, v170\n"
      "v_min3_f32 v176, v168, v169, v170\n"
      "v_max3_f32 v194, v171, v172, v194\n"
      "v_min3_f32 v176, v171, v172, v176\n"
      "v_max3_f32 v194, v173, v174, v194\n"
      "v_min3_f32 v176, v173, v174, v176\n"
      "v_max_f32 v194, v175, v194\n"
      "v_min_f32 v176, v175, v176\n"
      "v_max3_f32 v195, v180, v181, v182\n"
      "v_min3_f32 v188, v180, v181, v182\n"
      "v_max3_f32 v195, v183, v184, v195\n"
      "v_min3_f32 v188, v183, v184, v188\n"
      "v_max3_f32 v195, v185, v186, v195\n"
      "v_min3_f32 v188, v185, v186, v188\n"
      "v_max_f32 v195, v187, v195\n"
      "v_min_f32 v188, v187, v188\n"
      "v_min3_f32 v197, v152, v164, v176\n"
      "v_min_f32 v197, v188, v197\n"
      "v_permlane32_swap_b32 v192, v194\n"
      "v_permlane32_swap_b32 v193, v195\n"
      "v_cmp_class_f32 vcc, v197, 4\n"
      "v_max_f32 v192, v192, v194\n"
      "v_max_f32 v193, v193, v195\n"
      "s_nop 0\n"
      "s_or_b32 s51, s51, vcc_lo\n"
      "v_permlane16_swap_b32 v192, v193\n"
      "s_or_b32 s51, s51, vcc_hi\n"
      "v_max_f32 v192, v192, v193\n"
      "s_nop 0\n"
      "s_nop 0\n"
      "v_max_f32_dpp v192, v192, v192 row_ror:8 row_mask:0xf bank_mask:0xf\n"
      "s_nop 0\n"
      "s_nop 0\n"
      "v_max_f32_dpp v192, v192, v192 row_ror:4 row_mask:0xf bank_mask:0xf\n"
      "s_nop 0\n"
      "s_nop 0\n"
      "v_max_f32_dpp v192, v192, v192 row_ror:2 row_mask:0xf bank_mask:0xf\n"
      "s_nop 0\n"
      "s_nop 0\n"
      "v_max_f32_dpp v192, v192, v192 row_ror:1 row_mask:0xf bank_mask:0xf\n"
      "v_mul_f32 v192, v192, v204\n"
      "v_sub_f32 v192, 0, v192\n"
      "s_nop 0\n"
      "v_readlane_b32 s52, v204, 0\n"
      "v_readlane_b32 s94, v192, 0\n"
      "v_readlane_b32 s53, v204, 16\n"
      "v_readlane_b32 s95, v192, 16\n"
      "v_readlane_b32 s54, v204, 32\n"
      "v_readlane_b32 s96, v192, 32\n"
      "v_readlane_b32 s55, v204, 48\n"
      "v_readlane_b32 s97, v192, 48\n"
      "s_nop 1\n"
      "v_mov_b32 v192, s94\n"
      "v_mov_b32 v193, s95\n"
      "v_mov_b32 v194, s96\n"
      "v_mov_b32 v195, s97\n"
      "v_fma_f32 v144, v144, s52, v192\n"
      "v_fma_f32 v145, v145, s52, v192\n"
      "v_fma_f32 v146, v146, s52, v192\n"
      "v_fma_f32 v147, v147, s52, v192\n"
      "v_fma_f32 v148, v148, s52, v192\n"
      "v_fma_f32 v149, v149, s52, v192\n"
      "v_fma_f32 v150, v150, s52, v192\n"
      "v_fma_f32 v151, v151, s52, v192\n"
      "v_fma_f32 v156, v156, s53, v193\n"
      "v_fma_f32 v157, v157, s53, v193\n"
      "v_fma_f32 v158, v158, s53, v193\n"
      "v_fma_f32 v159, v159, s53, v193\n"
      "v_fma_f32 v160, v160, s53, v193\n"
      "v_fma_f32 v161, v161, s53, v193\n"
      "v_fma_f32 v162, v162, s53, v193\n"
      "v_fma_f32 v163, v163, s53, v193\n"
      "v_fma_f32 v168, v168, s54, v194\n"
      "v_fma_f32 v169, v169, s54, v194\n"
      "v_fma_f32 v170, v170, s54, v194\n"
      "v_fma_f32 v171, v171, s54, v194\n"
      "v_fma_f32 v172, v172, s54, v194\n"
      "v_fma_f32 v173, v173, s54, v194\n"
      "v_fma_f32 v174, v174, s54, v194\n"
      "v_fma_f32 v175, v175, s54, v194\n"
      "v_fma_f32 v180, v180, s55, v195\n"
      "v_fma_f32 v181, v181, s55, v195\n"
      "v_fma_f32 v182, v182, s55, v195\n"
      "v_fma_f32 v183, v183, s55, v195\n"
      "v_fma_f32 v184, v184, s55, v195\n"
      "v_fma_f32 v185, v185, s55, v195\n"
      "v_fma_f32 v186, v186, s55, v195\n"
      "v_fma_f32 v187, v187, s55, v195\n"
      "v_exp_f32 v144, v144\n"
      "v_exp_f32 v145, v145\n"
      "v_exp_f32 v146, v146\n"
      "v_exp_f32 v147, v147\n"
      "v_exp_f32 v148, v148\n"
      "v_exp_f32 v149, v149\n"
      "v_exp_f32 v150, v150\n"
      "v_exp_f32 v151, v151\n"
      "v_exp_f32 v156, v156\n"
      "v_exp_f32 v157, v157\n"
      "v_exp_f32 v158, v158\n"
      "v_exp_f32 v159, v159\n"
      "v_exp_f32 v160, v160\n"
      "v_exp_f32 v161, v161\n"
      "v_exp_f32 v162, v162\n"
      "v_exp_f32 v163, v163\n"
      "v_exp_f32 v168, v168\n"
      "v_exp_f32 v169, v169\n"
      "v_exp_f32 v170, v170\n"
      "v_exp_f32 v171, v171\n"
      "v_exp_f32 v172, v172\n"
      "v_exp_f32 v173, v173\n"
      "v_exp_f32 v174, v174\n"
      "v_exp_f32 v175, v175\n"
      "v_exp_f32 v180, v180\n"
      "v_exp_f32 v181, v181\n"
      "v_exp_f32 v182, v182\n"
      "v_exp_f32 v183, v183\n"
      "v_exp_f32 v184, v184\n"
      "v_exp_f32 v185, v185\n"
      "v_exp_f32 v186, v186\n"
      "v_exp_f32 v187, v187\n"
      "v_mul_f32 v208, v144, v32\n"
      "v_mul_f32 v209, v156, v44\n"
      "v_mul_f32 v210, v168, v56\n"
      "v_mul_f32 v211, v180, v68\n"
      "v_add_f32 v192, v144, v145\n"
      "v_add_f32 v193, v156, v157\n"
      "v_add_f32 v194, v168, v169\n"
      "v_add_f32 v195, v180, v181\n"
      "v_fmac_f32 v208, v145, v33\n"
      "v_fmac_f32 v209, v157, v45\n"
      "v_fmac_f32 v210, v169, v57\n"
      "v_fmac_f32 v211, v181, v69\n"
      "v_fmac_f32 v208, v146, v34\n"
      "v_fmac_f32 v209, v158, v46\n"
      "v_fmac_f32 v210, v170, v58\n"
      "v_fmac_f32 v211, v182, v70\n"
      "v_add_f32 v192, v146, v192\n"
      "v_add_f32 v193, v158, v193\n"
      "v_add_f32 v194, v170, v194\n"
      "v_add_f32 v195, v182, v195\n"
      "v_fmac_f32 v208, v147, v35\n"
      "v_fmac_f32 v209, v159, v47\n"
      "v_fmac_f32 v210, v171, v59\n"
      "v_fmac_f32 v211, v183, v71\n"
      "v_add_f32 v192, v147, v192\n"
      "v_add_f32 v193, v159, v193\n"
      "v_add_f32 v194, v171, v194\n"
      "v_add_f32 v195, v183, v195\n"
      "v_fmac_f32 v208, v148, v36\n"
      "v_fmac_f32 v209, v160, v48\n"
      "v_fmac_f32 v210, v172, v60\n"
      "v_fmac_f32 v211, v184, v72\n"
      "v_add_f32 v192, v148, v192\n"
      "v_add_f32 v193, v160, v193\n"
      "v_add_f32 v194, v172, v194\n"
      "v_add_f32 v195, v184, v195\n"
      "v_fmac_f32 v208, v149, v37\n"
      "v_fmac_f32 v209, v161, v49\n"
      "v_fmac_f32 v210, v173, v61\n"
      "v_fmac_f32 v211, v185, v73\n"
      "v_add_f32 v192, v149, v192\n"
      "v_add_f32 v193, v161, v193\n"
      "v_add_f32 v194, v173, v194\n"
      "v_add_f32 v195, v185, v195\n"
      "v_fmac_f32 v208, v150, v38\n"
      "v_fmac_f32 v209, v162, v50\n"
      "v_fmac_f32 v210, v174, v62\n"
      "v_fmac_f32 v211, v186, v74\n"
      "v_add_f32 v192, v150, v192\n"
      "v_add_f32 v193, v162, v193\n"
      "v_add_f32 v194, v174, v194\n"
      "v_add_f32 v195, v186, v195\n"
      "v_fmac_f32 v208, v151, v39\n"
      "v_fmac_f32 v209, v163, v51\n"
      "v_fmac_f32 v210, v175, v63\n"
      "v_fmac_f32 v211, v187, v75\n"
      "v_add_f32 v192, v151, v192\n"
      "v_add_f32 v193, v163, v193\n"
      "v_add_f32 v194, v175, v194\n"
      "v_add_f32 v195, v187, v195\n"
      "v_permlane32_swap_b32 v208, v210\n"
      "v_permlane32_swap_b32 v209, v211\n"
      "v_permlane32_swap_b32 v192, v194\n"
      "v_permlane32_swap_b32 v193, v195\n"
      "v_add_f32 v208, v208, v210\n"
      "v_add_f32 v209, v209, v211\n"
      "v_add_f32 v192, v192, v194\n"
      "v_add_f32 v193, v193, v195\n"
      "s_nop 0\n"
      "v_permlane16_swap_b32 v208, v209\n"
      "v_permlane16_swap_b32 v192, v193\n"
      "v_add_f32 v208, v208, v209\n"
      "v_add_f32 v192, v192, v193\n"
      "s_nop 0\n"
      "v_add_f32_dpp v208, v208, v208 row_ror:8 row_mask:0xf bank_mask:0xf\n"
      "v_add_f32_dpp v192, v192, v192 row_ror:8 row_mask:0xf bank_mask:0xf\n"
      "s_nop 0\n"
      "v_add_f32_dpp v208, v208, v208 row_ror:4 row_mask:0xf bank_mask:0xf\n"
      "v_add_f32_dpp v192, v192, v192 row_ror:4 row_mask:0xf bank_mask:0xf\n"
      "s_nop 0\n"
      "v_add_f32_dpp v208, v208, v208 row_ror:2 row_mask:0xf bank_mask:0xf\n"
      "v_add_f32_dpp v192, v192, v192 row_ror:2 row_mask:0xf bank_mask:0xf\n"
      "s_nop 0\n"
      "v_add_f32_dpp v208, v208, v208 row_ror:1 row_mask:0xf bank_mask:0xf\n"
      "v_add_f32_dpp v192, v192, v192 row_ror:1 row_mask:0xf bank_mask:0xf\n"
      "s_nop 0\n"
      "v_rcp_f32 v192, v192\n"
      "v_mul_f32 v208, v208, v205\n"
      "v_mov_b32 v202, 0x207\n"
      "v_mul_f32 v208, v208, v192\n"
      "v_cmp_class_f32 vcc, v208, v202\n"
      "v_cvt_pk_bf16_f32 v208, v208, v208\n"
      "s_or_b32 s51, s51, vcc_lo\n"
      "s_or_b32 s51, s51, vcc_hi\n"
      "s_mov_b64 s[98:99], exec\n"
      "s_mov_b32 exec_lo, 0x00010001\n"
      "s_mov_b32 exec_hi, 0x00010001\n"
      "global_store_short v206, v208, s[48:49]\n"
      "s_mov_b64 exec, s[98:99]\n"
      "s_branch .Lfin_%=\n"
      ".Lsigzero_%=:\n"
      "s_waitcnt vmcnt(8)\n"
      "v_mov_b32 v245, 0\n"
      "v_lshlrev_b32 v246, 16, v96\n"
      "v_max3_f32 v245, |v246|, |v96|, v245\n"
      "v_lshlrev_b32 v246, 16, v97\n"
      "v_max3_f32 v245, |v246|, |v97|, v245\n"
      "v_lshlrev_b32 v246, 16, v98\n"
      "v_max3_f32 v245, |v246|, |v98|, v245\n"
      "v_lshlrev_b32 v246, 16, v99\n"
      "v_max3_f32 v245, |v246|, |v99|, v245\n"
      "v_lshlrev_b32 v246, 16, v100\n"
      "v_max3_f32 v245, |v246|, |v100|, v245\n"
      "v_lshlrev_b32 v246, 16, v101\n"
      "v_max3_f32 v245, |v246|, |v101|, v245\n"
      "v_lshlrev_b32 v246, 16, v102\n"
      "v_max3_f32 v245, |v246|, |v102|, v245\n"
      "v_lshlrev_b32 v246, 16, v103\n"
      "v_max3_f32 v245, |v246|, |v103|, v245\n"
      "v_lshlrev_b32 v246, 16, v104\n"
      "v_max3_f32 v245, |v246|, |v104|, v245\n"
      "v_lshlrev_b32 v246, 16, v105\n"
      "v_max3_f32 v245, |v246|, |v105|, v245\n"
      "v_lshlrev_b32 v246, 16, v106\n"
      "v_max3_f32 v245, |v246|, |v106|, v245\n"
      "v_lshlrev_b32 v246, 16, v107\n"
      "v_max3_f32 v245, |v246|, |v107|, v245\n"
      "v_lshlrev_b32 v246, 16, v108\n"
      "v_max3_f32 v245, |v246|, |v108|, v245\n"
      "v_lshlrev_b32 v246, 16, v109\n"
      "v_max3_f32 v245, |v246|, |v109|, v245\n"
      "v_lshlrev_b32 v246, 16, v110\n"
      "v_max3_f32 v245, |v246|, |v110|, v245\n"
      "v_lshlrev_b32 v246, 16, v111\n"
      "v_max3_f32 v245, |v246|, |v111|, v245\n"
      "s_nop 1\n"
      "v_max_f32_dpp v245, v245, v245 row_ror:8 row_mask:0xf bank_mask:0xf\n"
      "s_nop 1\n"
      "v_max_f32_dpp v245, v245, v245 row_ror:4 row_mask:0xf bank_mask:0xf\n"
      "s_nop 1\n"
      "v_max_f32_dpp v245, v245, v245 row_ror:2 row_mask:0xf bank_mask:0xf\n"
      "s_nop 1\n"
      "v_max_f32_dpp v245, v245, v245 row_ror:1 row_mask:0xf bank_mask:0xf\n"
      "v_cmp_gt_u32 vcc, 0x00800000, v242\n"
      "s_nop 1\n"
      "v_cndmask_b32 v242, v242, v245, vcc\n"
      "v_mov_b32 v245, v242\n"
      "v_mov_b32 v246, v245\n"
      "s_nop 1\n"
      "v_permlane32_swap_b32 v245, v246\n"
      "s_nop 1\n"
      "v_max_f32 v245, v245, v246\n"
      "v_mov_b32 v246, v245\n"
      "s_nop 1\n"
      "v_permlane16_swap_b32 v245, v246\n"
      "s_nop 1\n"
      "v_max_f32 v245, v245, v246\n"
      "v_cmp_gt_u32 vcc, 0x00800000, v242\n"
      "s_nop 1\n"
      "v_cndmask_b32 v242, v242, v245, vcc\n"
      "s_branch .Lsig_%=\n"
      ".Lfin_%=:\n"
      "s_mov_b32 %0, s51\n"
      ".Lend_%=:\n"
      : "=s"(flag)
      : "s"(__builtin_amdgcn_kernarg_segment_ptr()), "s"(blk), "s"(w), "s"(ldsbase)
      : "v0", "v1", "v2", "v3", "v4", "v5", "v6", "v7", "v8", "v9", "v10", "v11", "v12", "v13", "v14", "v15", "v16", "v17", "v18", "v19", "v20", "v21", "v22", "v23", "v24", "v25", "v26", "v27", "v28", "v29", "v30", "v31", "v32", "v33", "v34", "v35", "v36", "v37", "v38", "v39", "v40", "v41", "v42", "v43", "v44", "v45", "v46", "v47", "v48", "v49", "v50", "v51", "v52", "v53", "v54", "v55", "v56", "v57", "v58", "v59", "v60", "v61", "v62", "v63", "v64", "v65", "v66", "v67", "v68", "v69", "v70", "v71", "v72", "v73", "v74", "v75", "v76", "v77", "v78", "v79", "v80", "v81", "v82", "v83", "v84", "v85", "v86", "v87", "v88", "v89", "v90", "v91", "v92", "v93", "v94", "v95", "v96", "v97", "v98", "v99", "v100", "v101", "v102", "v103", "v104", "v105", "v106", "v107", "v108", "v109", "v110", "v111", "v112", "v113", "v114", "v115", "v116", "v117", "v118", "v119", "v120", "v121", "v122", "v123", "v124", "v125", "v126", "v127", "v128", "v129", "v130", "v131", "v132", "v133", "v134", "v135", "v136", "v137", "v138", "v139", "v140", "v141", "v142", "v143", "v144", "v145", "v146", "v147", "v148", "v149", "v150", "v151", "v152", "v153", "v154", "v155", "v156", "v157", "v158", "v159", "v160", "v161", "v162", "v163", "v164", "v165", "v166", "v167", "v168", "v169", "v170", "v171", "v172", "v173", "v174", "v175", "v176", "v177", "v178", "v179", "v180", "v181", "v182", "v183", "v184", "v185", "v186", "v187", "v188", "v189", "v190", "v191", "v192", "v193", "v194", "v195", "v196", "v197", "v198", "v199", "v200", "v201", "v202", "v203", "v204", "v205", "v206", "v207", "v208", "v209", "v210", "v211", "v212", "v213", "v214", "v215", "v216", "v217", "v218", "v219", "v220", "v221", "v222", "v223", "v224", "v225", "v226", "v227", "v228", "v229", "v230", "v231", "v232", "v233", "v234", "v235", "v236", "v237", "v238", "v239", "v240", "v241", "v242", "v243", "v244", "v245", "v246", "v247", "v248", "v249", "v250", "v251", "v252", "v253", "v254", "v255", "a0", "a1", "a2", "a3", "a4", "a5", "a6", "a7", "a8", "a9", "a10", "a11", "a12", "a13", "a14", "a15", "a16", "a17", "a18", "a19", "a20", "a21", "a22", "a23", "a24", "a25", "a26", "a27", "a28", "a29", "a30", "a31", "a32", "a33", "a34", "a35", "a36", "a37", "a38", "a39", "a40", "a41", "a42", "a43", "a44", "a45", "a46", "a47", "a48", "a49", "a50", "a51", "a52", "a53", "a54", "a55", "a56", "a57", "a58", "a59", "a60", "a61", "a62", "a63", "a64", "a65", "a66", "a67", "a68", "a69", "a70", "a71", "a72", "a73", "a74", "a75", "a76", "a77", "a78", "a79", "a80", "a81", "a82", "a83", "a84", "a85", "a86", "a87", "a88", "a89", "a90", "a91", "a92", "a93", "a94", "a95", "a96", "a97", "a98", "a99", "a100", "a101", "a102", "a103", "a104", "a105", "a106", "a107", "a108", "a109", "a110", "a111", "a112", "a113", "a114", "a115", "a116", "a117", "a118", "a119", "a120", "a121", "a122", "a123", "a124", "a125", "a126", "a127", "a128", "a129", "a130", "a131", "a132", "a133", "a134", "a135", "a136", "a137", "a138", "a139", "a140", "a141", "a142", "a143", "a144", "a145", "a146", "a147", "a148", "a149", "a150", "a151", "a152", "a153", "a154", "a155", "a156", "a157", "a158", "a159", "a160", "a161", "a162", "a163", "a164", "a165", "a166", "a167", "a168", "a169", "a170", "a171", "a172", "a173", "a174", "a175", "a176", "a177", "a178", "a179", "a180", "a181", "a182", "a183", "s36", "s37", "s38", "s39", "s40", "s41", "s42", "s43", "s44", "s45", "s46", "s47", "s48", "s49", "s50", "s51", "s52", "s53", "s54", "s55", "s56", "s57", "s58", "s59", "s60", "s61", "s62", "s63", "s64", "s65", "s66", "s67", "s68", "s69", "s70", "s71", "s72", "s73", "s74", "s75", "s76", "s77", "s78", "s79", "s80", "s81", "s82", "s83", "s84", "s85", "s86", "s87", "s88", "s89", "s90", "s91", "s92", "s93", "s94", "s95", "s96", "s97", "s98", "s99", "vcc", "scc", "memory");
  if (flag) afm_general(idx + (blk * 16 + w * 4) * 26, table, W, bv, hv, pv, out + blk * 16 + w * 4, threadIdx.x & 63,
                        (float*)(lds + w * (4 * 26 * 272)));
}

extern "C" void kernel_launch(void* const* d_in, const int* in_sizes, int n_in,
                              void* d_out, int out_size, void* d_ws, size_t ws_size,
                              hipStream_t stream) {
  const int B = out_size;
  afm_fused<<<B / 16, 256, 0, stream>>>((const int*)d_in[0], (const unsigned short*)d_in[1], (const unsigned short*)d_in[4],
                                        (const unsigned short*)d_in[5], (const unsigned short*)d_in[6], (const unsigned short*)d_in[7],
                                        (unsigned short*)d_out, (unsigned long long)(((size_t)in_sizes[1] * 2u / 256u) & ~(size_t)3));
}
```
